```python
import torch

def _softmax(x, axis=-1):
    return torch.softmax(x, dim=axis)
def _gelu(x):
    return torch.nn.functional.gelu(x, approximate="tanh")
def _silu(x):
    return torch.nn.functional.silu(x)
def _sigmoid(x):
    return torch.sigmoid(x)
def _layernorm(x, axis=-1, eps=1e-5):
    m = x.mean(dim=axis, keepdim=True)
    v = x.var(dim=axis, keepdim=True, unbiased=False)
    return (x - m) / torch.sqrt(v + eps)
def _scatter_add(tgt, idx, src, axis=0):
    return tgt.clone().index_add_(axis, idx, src)
def _segment_sum(src, seg, n):
    out = torch.zeros((n,) + src.shape[1:], dtype=src.dtype)
    return out.index_add_(0, seg, src)
def _pad(x, pw):
    flat = tuple(v for pair in reversed(pw) for v in pair)
    return torch.nn.functional.pad(x, flat)

kernel_name = 'standalone__swiglu_ffn__sampling'

def setup_inputs():

    _w = (lambda a: (lambda t: t.to(torch.bfloat16) if t.is_floating_point() else t)(torch.as_tensor(a)))
    B, S, D, F = 32, 32, 1024, 3584
    return {
        "x": _w((lambda *s: torch.randn(*s, dtype=torch.bfloat16) * 0.05)(B, S, D)),
        "w_gate": _w((lambda *s: torch.randn(*s, dtype=torch.bfloat16) * 0.05)(D, F)),
        "w_up": _w((lambda *s: torch.randn(*s, dtype=torch.bfloat16) * 0.05)(D, F)),
        "w_down": _w((lambda *s: torch.randn(*s, dtype=torch.bfloat16) * 0.05)(F, D)),
    }

def reference(x, w_gate, w_up, w_down):
    g = _silu(x @ w_gate)
    u = x @ w_up
    return (g * u) @ w_down
```

```cpp
#include <hip/hip_runtime.h>
#include <hip/hip_bf16.h>

typedef short s16x4 __attribute__((ext_vector_type(4)));
typedef short s16x8 __attribute__((ext_vector_type(8)));
typedef __bf16 bf16x8 __attribute__((ext_vector_type(8)));
typedef __bf16 bf16x2 __attribute__((ext_vector_type(2)));
typedef float f32x4 __attribute__((ext_vector_type(4)));
typedef float f32x2 __attribute__((ext_vector_type(2)));
typedef unsigned u32x4 __attribute__((ext_vector_type(4)));
typedef unsigned u32x2 __attribute__((ext_vector_type(2)));
#define LAS __attribute__((address_space(3)))
#define GAS __attribute__((address_space(1)))

constexpr int MTOK = 1024, DM = 1024, FF = 3584;

template <int N> __device__ __forceinline__ void wait_vm() { asm volatile("s_waitcnt vmcnt(%0)" ::"i"(N) : "memory"); }
__device__ __forceinline__ void wait_lgkm0() { asm volatile("s_waitcnt lgkmcnt(0)" ::: "memory"); }
__device__ __forceinline__ void barrier() {
    asm volatile("" ::: "memory");
    __builtin_amdgcn_s_barrier();
    asm volatile("" ::: "memory");
}
__device__ __forceinline__ bf16x8 lds_b128(LAS char* p) { return *(LAS bf16x8*)p; }
__device__ __forceinline__ bf16x8 lds_tr2(LAS char* p0, LAS char* p1) {
    s16x4 a = __builtin_amdgcn_ds_read_tr16_b64_v4i16((LAS s16x4*)p0);
    s16x4 b = __builtin_amdgcn_ds_read_tr16_b64_v4i16((LAS s16x4*)p1);
    return __builtin_bit_cast(bf16x8, __builtin_shufflevector(a, b, 0, 1, 2, 3, 4, 5, 6, 7));
}
__device__ __forceinline__ unsigned pack_bf16(float a, float b) {
    bf16x2 v = {(__bf16)a, (__bf16)b};
    return __builtin_bit_cast(unsigned, v);
}
__device__ __forceinline__ float swiglu(float g, float u) {
    float e = __builtin_amdgcn_exp2f(g * -1.44269504088896341f);
    return g * u * __builtin_amdgcn_rcpf(1.0f + e);
}
#define SGB(mask, n) __builtin_amdgcn_sched_group_barrier(mask, n, 0)

constexpr int G1_STAGE = 48 * 1024, G1_NK = 16;

__global__ __launch_bounds__(512, 2) void ffn_g1(const char* __restrict__ x, const char* __restrict__ wg,
                                                 const char* __restrict__ wu, char* __restrict__ h,
                                                 unsigned* __restrict__ cnt) {
    extern __shared__ __attribute__((aligned(16))) char smem_[];
    LAS char* smem = (LAS char*)smem_;
    const int tid = threadIdx.x, lane = tid & 63;
    const int wid = __builtin_amdgcn_readfirstlane(tid >> 6);
    const int b = blockIdx.x;
    const int t = (b & 7) * 28 + (b >> 3);
    const int fb = t >> 3, mb = t & 7;
    const int rot = (2 * mb + 4 * (fb & 3) + 2 * ((fb >> 2) & 1)) & 15;
    if (b == 0 && tid < 64) cnt[tid * 64] = 0;

    if (wid >= 4) {
        const int p = wid - 4;
        auto rx = __builtin_amdgcn_make_buffer_rsrc((void*)x, 0, MTOK * DM * 2, 0x00020000);
        auto rg = __builtin_amdgcn_make_buffer_rsrc((void*)wg, 0, DM * FF * 2, 0x00020000);
        auto ru = __builtin_amdgcn_make_buffer_rsrc((void*)wu, 0, DM * FF * 2, 0x00020000);
        unsigned vx[2], vw[2];
#pragma unroll
        for (int e = 0; e < 2; ++e) {
            int r = 8 * e + (lane >> 3);
            vx[e] = r * 2048 + (((lane & 7) ^ ((r >> 1) & 7)) << 4);
            int f = (lane >> 4) | (e << 2);
            vw[e] = (lane >> 4) * (FF * 2) + (((lane & 15) ^ (f << 1)) << 4);
        }
        const unsigned xbase = (mb * 128 + p * 32) * 2048;
        const unsigned wbase = (p * 16) * (FF * 2) + fb * 256;
        auto stage = [&](int s) {
            int kk = (s + rot) & 15;
            LAS char* dst = smem + (s % 3) * G1_STAGE + p * 4096;
            unsigned sx = xbase + kk * 128;
            unsigned sw = wbase + kk * 64 * (FF * 2);
#pragma unroll
            for (int i = 0; i < 4; ++i) {
                __builtin_amdgcn_raw_ptr_buffer_load_lds(rg, (LAS void*)(dst + 16384 + i * 1024), 16, vw[i >> 1], sw + i * 4 * (FF * 2), 0, 0);
                __builtin_amdgcn_raw_ptr_buffer_load_lds(ru, (LAS void*)(dst + 32768 + i * 1024), 16, vw[i >> 1], sw + i * 4 * (FF * 2), 0, 0);
                __builtin_amdgcn_raw_ptr_buffer_load_lds(rx, (LAS void*)(dst + i * 1024), 16, vx[i & 1], sx + (i >> 1) * 16 * 2048, 0, 0);
            }
        };
        stage(0); stage(1); stage(2);
        wait_vm<24>();
        barrier();
        wait_vm<12>();
        barrier();
        for (int s = 3; s < G1_NK; ++s) {
            stage(s);
            wait_vm<12>();
            barrier();
        }
        wait_vm<0>();
        barrier();
    } else {
        const int wm = wid >> 1, wn = wid & 1;
        const int fr = lane & 15, g = lane >> 4, q = (lane & 15) >> 2, pp = lane & 3;
        unsigned ax[2], aw[4];
#pragma unroll
        for (int ks = 0; ks < 2; ++ks) ax[ks] = (wm * 64 + fr) * 128 + ((((ks * 4 + g) ^ (fr >> 1)) & 7) << 4);
        const int fsw = q | ((g & 1) << 2);
#pragma unroll
        for (int nt = 0; nt < 4; ++nt) aw[nt] = 16384 + (8 * g + q) * 256 + (((wn * 4 + nt) ^ fsw) << 5) + pp * 8;
        f32x4 acc[4][8];
#pragma unroll
        for (int i = 0; i < 4; ++i)
#pragma unroll
            for (int j = 0; j < 8; ++j) acc[i][j] = (f32x4){0.f, 0.f, 0.f, 0.f};
        bf16x8 xa[4], wa[8], xb[4], wb[8];
        auto rd = [&](bf16x8(&xf)[4], bf16x8(&wf)[8], LAS char* st, int ks) {
#pragma unroll
            for (int nt = 0; nt < 8; ++nt) {
                LAS char* pw = st + aw[nt & 3] + (nt >> 2) * 16384 + ks * 8192;
                wf[nt] = lds_tr2(pw, pw + 1024);
                if (nt < 4) xf[nt] = lds_b128(st + ax[ks] + nt * 2048);
            }
        };
        auto mm = [&](bf16x8(&xf)[4], bf16x8(&wf)[8]) {
#pragma unroll
            for (int nt = 0; nt < 8; ++nt)
#pragma unroll
                for (int mt = 0; mt < 4; ++mt) acc[mt][nt] = __builtin_amdgcn_mfma_f32_16x16x32_bf16(wf[nt], xf[mt], acc[mt][nt], 0, 0, 0);
        };
        auto sched = [&]() {
#pragma unroll
            for (int i = 0; i < 20; ++i) { SGB(0x100, 1); SGB(0x8, 1); }
            SGB(0x8, 12);
        };
        barrier();
        rd(xa, wa, smem, 0);
        int slot = 0;
        for (int s = 0; s < G1_NK - 1; ++s) {
            LAS char* st = smem + slot * G1_STAGE;
            rd(xb, wb, st, 1);
            mm(xa, wa);
            sched();
            __builtin_amdgcn_sched_barrier(0);
            wait_lgkm0();
            barrier();
            __builtin_amdgcn_sched_barrier(0);
            slot = slot == 2 ? 0 : slot + 1;
            rd(xa, wa, smem + slot * G1_STAGE, 0);
            mm(xb, wb);
            sched();
            __builtin_amdgcn_sched_barrier(0);
        }
        rd(xb, wb, smem + slot * G1_STAGE, 1);
        mm(xa, wa);
        mm(xb, wb);
        char* hp = h + ((size_t)(mb * 56 + fb * 2 + wn) * 16 + wm * 8) * 1024 + lane * 16;
#pragma unroll
        for (int mt = 0; mt < 4; ++mt)
#pragma unroll
            for (int s2 = 0; s2 < 2; ++s2) {
                f32x4 g0 = acc[mt][2 * s2], g1 = acc[mt][2 * s2 + 1], u0 = acc[mt][4 + 2 * s2], u1 = acc[mt][5 + 2 * s2];
                u32x4 o;
                o[0] = pack_bf16(swiglu(g0[0], u0[0]), swiglu(g0[1], u0[1]));
                o[1] = pack_bf16(swiglu(g0[2], u0[2]), swiglu(g0[3], u0[3]));
                o[2] = pack_bf16(swiglu(g1[0], u1[0]), swiglu(g1[1], u1[1]));
                o[3] = pack_bf16(swiglu(g1[2], u1[2]), swiglu(g1[3], u1[3]));
                *(u32x4*)(hp + (mt * 2 + s2) * 1024) = o;
            }
    }
}

constexpr int G2_STAGE = 32 * 1024, G2_NK = 14, G2_NST = 5;

__global__ __launch_bounds__(512, 2) void ffn_g2(const char* __restrict__ h, const char* __restrict__ wd,
                                                 char* __restrict__ out, char* __restrict__ part,
                                                 unsigned* __restrict__ cnt) {
    extern __shared__ __attribute__((aligned(16))) char smem_[];
    LAS char* smem = (LAS char*)smem_;
    const int tid = threadIdx.x, lane = tid & 63;
    const int wid = __builtin_amdgcn_readfirstlane(tid >> 6);
    const int b = blockIdx.x;
    const int xcd = b & 7, idx = b >> 3;
    const int mb = idx & 7, nb = (xcd & 1) * 4 + (idx >> 3), kq = xcd >> 1;
    const int rot = (mb * 14 / 8 + (idx >> 3) * 14 / 4) % 14;
    const int tile = mb * 8 + nb;

    if (wid >= 4) {
        const int p = wid - 4;
        auto rh = __builtin_amdgcn_make_buffer_rsrc((void*)h, 0, MTOK * FF * 2, 0x00020000);
        auto rw = __builtin_amdgcn_make_buffer_rsrc((void*)wd, 0, DM * FF * 2, 0x00020000);
        const unsigned vh = lane * 16;
        unsigned vw[2];
#pragma unroll
        for (int e = 0; e < 2; ++e) {
            int f = (lane >> 4) | (e << 2);
            vw[e] = (lane >> 4) * (DM * 2) + (((lane & 15) ^ (f << 1)) << 4);
        }
        const unsigned hbase = (mb * 56 + kq * 14) * 16384 + p * 4096;
        const unsigned wbase = (kq * 896 + p * 16) * (DM * 2) + nb * 256;
        auto stage = [&](int s) {
            int kk = s + rot; if (kk >= G2_NK) kk -= G2_NK;
            LAS char* dst = smem + (s % G2_NST) * G2_STAGE + p * 4096;
            unsigned sh = hbase + kk * 16384;
            unsigned sw = wbase + kk * 64 * (DM * 2);
#pragma unroll
            for (int i = 0; i < 4; ++i) {
                __builtin_amdgcn_raw_ptr_buffer_load_lds(rw, (LAS void*)(dst + 16384 + i * 1024), 16, vw[i & 1], sw + i * 4 * (DM * 2), 0, 0);
                __builtin_amdgcn_raw_ptr_buffer_load_lds(rh, (LAS void*)(dst + i * 1024), 16, vh, sh + i * 1024, 0, 0);
            }
        };
        stage(0); stage(1); stage(2); stage(3); stage(4);
        wait_vm<32>();
        barrier();
        wait_vm<24>();
        barrier();
        for (int s = 5; s < G2_NK; ++s) {
            stage(s);
            wait_vm<24>();
            barrier();
        }
        wait_vm<16>(); barrier();
        wait_vm<8>(); barrier();
        wait_vm<0>(); barrier();
        barrier();
        barrier();
    } else {
        const int wm = wid >> 1, wn = wid & 1;
        const int g = lane >> 4, q = (lane & 15) >> 2, pp = lane & 3;
        const unsigned ah = wm * 8192 + lane * 16;
        unsigned aw[4];
        const int fsw = q | ((g & 1) << 2);
#pragma unroll
        for (int nt = 0; nt < 4; ++nt) aw[nt] = 16384 + (4 * g + q) * 256 + (((wn * 4 + nt) ^ fsw) << 5) + pp * 8;
        f32x4 acc[4][4];
#pragma unroll
        for (int i = 0; i < 4; ++i)
#pragma unroll
            for (int j = 0; j < 4; ++j) acc[i][j] = (f32x4){0.f, 0.f, 0.f, 0.f};
        bf16x8 xa[4], wa[4], xb[4], wb[4];
        auto rd = [&](bf16x8(&xf)[4], bf16x8(&wf)[4], LAS char* st, int ks) {
#pragma unroll
            for (int nt = 0; nt < 4; ++nt) {
                LAS char* pw = st + aw[nt] + ks * 8192;
                wf[nt] = lds_tr2(pw, pw + 4096);
                xf[nt] = lds_b128(st + ah + nt * 2048 + ks * 1024);
            }
        };
        auto mm = [&](bf16x8(&xf)[4], bf16x8(&wf)[4]) {
#pragma unroll
            for (int nt = 0; nt < 4; ++nt)
#pragma unroll
                for (int mt = 0; mt < 4; ++mt) acc[mt][nt] = __builtin_amdgcn_mfma_f32_16x16x32_bf16(wf[nt], xf[mt], acc[mt][nt], 0, 0, 0);
        };
        auto sched = [&]() {
#pragma unroll
            for (int i = 0; i < 12; ++i) { SGB(0x100, 1); SGB(0x8, 1); }
            SGB(0x8, 4);
        };
        barrier();
        rd(xa, wa, smem, 0);
        int slot = 0;
        for (int s = 0; s < G2_NK - 1; ++s) {
            LAS char* st = smem + slot * G2_STAGE;
            rd(xb, wb, st, 1);
            mm(xa, wa);
            sched();
            __builtin_amdgcn_sched_barrier(0);
            wait_lgkm0();
            barrier();
            __builtin_amdgcn_sched_barrier(0);
            slot = slot == G2_NST - 1 ? 0 : slot + 1;
            rd(xa, wa, smem + slot * G2_STAGE, 0);
            mm(xb, wb);
            sched();
            __builtin_amdgcn_sched_barrier(0);
        }
        rd(xb, wb, smem + slot * G2_STAGE, 1);
        mm(xa, wa);
        mm(xb, wb);
        auto rp = __builtin_amdgcn_make_buffer_rsrc((void*)part, 0, 64 * 16 * 16384, 0x00020000);
        const unsigned pl = wid * 4096 + lane * 16;
#pragma unroll
        for (int mt = 0; mt < 4; ++mt) {
            if (mt != kq) {
#pragma unroll
                for (int nt = 0; nt < 4; ++nt)
                    __builtin_amdgcn_raw_buffer_store_b128(__builtin_bit_cast(u32x4, acc[mt][nt]), rp, pl + nt * 1024,
                                                           ((tile * 4 + kq) * 4 + mt) * 16384, 16);
            }
        }
        wait_vm<0>();
        barrier();
        if (wid == 0) {
            unsigned* c = cnt + tile * 64;
            if (lane == 0) __hip_atomic_fetch_add(c, 1u, __ATOMIC_RELAXED, __HIP_MEMORY_SCOPE_AGENT);
            unsigned spins = 0;
            while (__builtin_amdgcn_readfirstlane(__hip_atomic_load(c, __ATOMIC_RELAXED, __HIP_MEMORY_SCOPE_AGENT)) < 4u) {
                __builtin_amdgcn_s_sleep(1);
                if (++spins > 4000000u) break;
            }
        }
        barrier();
        f32x4 r[4];
#pragma unroll
        for (int mt = 0; mt < 4; ++mt)
            if (mt == kq) {
#pragma unroll
                for (int nt = 0; nt < 4; ++nt) r[nt] = acc[mt][nt];
            }
        u32x4 v[3][4];
#pragma unroll
        for (int j = 0; j < 3; ++j) {
            int src = (kq + 1 + j) & 3;
#pragma unroll
            for (int nt = 0; nt < 4; ++nt)
                v[j][nt] = __builtin_amdgcn_raw_buffer_load_b128(rp, pl + nt * 1024, ((tile * 4 + src) * 4 + kq) * 16384, 16);
        }
#pragma unroll
        for (int j = 0; j < 3; ++j)
#pragma unroll
            for (int nt = 0; nt < 4; ++nt) r[nt] += __builtin_bit_cast(f32x4, v[j][nt]);
        char* op = out + (size_t)(mb * 128 + wm * 64 + kq * 16 + (lane & 15)) * (DM * 2) + (nb * 128 + wn * 64 + g * 4) * 2;
#pragma unroll
        for (int nt = 0; nt < 4; ++nt) {
            u32x2 o = {pack_bf16(r[nt][0], r[nt][1]), pack_bf16(r[nt][2], r[nt][3])};
            *(u32x2*)(op + nt * 32) = o;
        }
    }
}

extern "C" void kernel_launch(void* const* d_in, const int* in_sizes, int n_in, void* d_out, int out_size, void* d_ws,
                              size_t ws_size, hipStream_t stream) {
    const char* x = (const char*)d_in[0];
    const char* wg = (const char*)d_in[1];
    const char* wu = (const char*)d_in[2];
    const char* wd = (const char*)d_in[3];
    char* ws = (char*)d_ws;
    unsigned* cnt = (unsigned*)ws;
    char* h = ws + 65536;
    char* part = ws + 65536 + (8u << 20);
    (void)hipFuncSetAttribute((const void*)ffn_g1, hipFuncAttributeMaxDynamicSharedMemorySize, 3 * G1_STAGE);
    (void)hipFuncSetAttribute((const void*)ffn_g2, hipFuncAttributeMaxDynamicSharedMemorySize, G2_NST * G2_STAGE);
    ffn_g1<<<224, 512, 3 * G1_STAGE, stream>>>(x, wg, wu, h, cnt);
    ffn_g2<<<256, 512, G2_NST * G2_STAGE, stream>>>(h, wd, (char*)d_out, part, cnt);
}
```

```cpp
#include <hip/hip_runtime.h>
#include <hip/hip_bf16.h>

typedef short s16x4 __attribute__((ext_vector_type(4)));
typedef short s16x8 __attribute__((ext_vector_type(8)));
typedef __bf16 bf16x8 __attribute__((ext_vector_type(8)));
typedef __bf16 bf16x2 __attribute__((ext_vector_type(2)));
typedef float f32x4 __attribute__((ext_vector_type(4)));
typedef float f32x2 __attribute__((ext_vector_type(2)));
typedef unsigned u32x4 __attribute__((ext_vector_type(4)));
typedef unsigned u32x2 __attribute__((ext_vector_type(2)));
#define LAS __attribute__((address_space(3)))
#define GAS __attribute__((address_space(1)))

constexpr int MTOK = 1024, DM = 1024, FF = 3584;

template <int N> __device__ __forceinline__ void wait_vm() { asm volatile("s_waitcnt vmcnt(%0)" ::"i"(N) : "memory"); }
__device__ __forceinline__ void wait_lgkm0() { __builtin_amdgcn_s_waitcnt(0xC07F); }
__device__ __forceinline__ void barrier() {
    asm volatile("" ::: "memory");
    __builtin_amdgcn_s_barrier();
    asm volatile("" ::: "memory");
}
__device__ __forceinline__ bf16x8 lds_b128(unsigned p) { return *(LAS bf16x8*)(size_t)p; }
__device__ __forceinline__ bf16x8 lds_tr2(unsigned p0, unsigned p1) {
    s16x4 a = __builtin_amdgcn_ds_read_tr16_b64_v4i16((LAS s16x4*)(size_t)p0);
    s16x4 b = __builtin_amdgcn_ds_read_tr16_b64_v4i16((LAS s16x4*)(size_t)p1);
    return __builtin_bit_cast(bf16x8, __builtin_shufflevector(a, b, 0, 1, 2, 3, 4, 5, 6, 7));
}
__device__ __forceinline__ unsigned pack_bf16(float a, float b) {
    bf16x2 v = {(__bf16)a, (__bf16)b};
    return __builtin_bit_cast(unsigned, v);
}
__device__ __forceinline__ float swiglu(float g, float u) {
    float e = __builtin_amdgcn_exp2f(g * -1.44269504088896341f);
    return g * u * __builtin_amdgcn_rcpf(1.0f + e);
}
#define TSDECL
#define TS(i)
#define TSFLUSH(base, n)
#define SGB(mask, n) __builtin_amdgcn_sched_group_barrier(mask, n, 0)
#define SB() __builtin_amdgcn_sched_barrier(0)

constexpr int G1_STAGE = 48 * 1024, G1_NK = 16;

__global__ __launch_bounds__(512, 2) void ffn_g1(const char* __restrict__ x, const char* __restrict__ wg,
                                                 const char* __restrict__ wu, char* __restrict__ h,
                                                 unsigned* __restrict__ cnt) {
    extern __shared__ __attribute__((aligned(16))) char smem_[];
    LAS char* smem = (LAS char*)smem_;
    const int tid = threadIdx.x, lane = tid & 63;
    const int wid = __builtin_amdgcn_readfirstlane(tid >> 6);
    TSDECL; TS(0);
    const int b = blockIdx.x;
    const int t = (b & 7) * 28 + (b >> 3);
    const int fb = t >> 3, mb = t & 7;
#define ROT1 5
    const int rot = ROT1 == 0 ? 0 : ROT1 == 1 ? (mb & 1) * 8 : ROT1 == 2 ? (mb & 3) * 4 : ROT1 == 3 ? (2 * mb + 4 * (fb & 3) + 2 * ((fb >> 2) & 1)) & 15 : ROT1 == 4 ? 2 * mb : ROT1 == 5 ? (2 * (mb + (b & 7))) & 15 : (4 * (mb + (b & 7))) & 15;
    if (b == 0 && tid < 64) cnt[tid * 64] = 0;

    if (wid >= 4) {
        const int p = wid - 4;
        auto rx = __builtin_amdgcn_make_buffer_rsrc((void*)x, 0, MTOK * DM * 2, 0x00020000);
        auto rg = __builtin_amdgcn_make_buffer_rsrc((void*)wg, 0, DM * FF * 2, 0x00020000);
        auto ru = __builtin_amdgcn_make_buffer_rsrc((void*)wu, 0, DM * FF * 2, 0x00020000);
        unsigned vx[2], vw[2];
#pragma unroll
        for (int e = 0; e < 2; ++e) {
            int r = 8 * e + (lane >> 3);
            vx[e] = r * 2048 + (((lane & 7) ^ ((r >> 1) & 7)) << 4);
            int f = (lane >> 4) | (e << 2);
            vw[e] = (lane >> 4) * (FF * 2) + (((lane & 15) ^ (f << 1)) << 4);
        }
        const unsigned xbase = (mb * 128 + p * 32) * 2048;
        const unsigned wbase = (p * 16) * (FF * 2) + fb * 256;
        auto stage = [&](int s) {
            int kk = (s + rot) & 15;
            LAS char* dst = smem + (s % 3) * G1_STAGE + p * 4096;
            unsigned sx = xbase + kk * 128;
            unsigned sw = wbase + kk * 64 * (FF * 2);
#pragma unroll
            for (int i = 0; i < 4; ++i) {
                __builtin_amdgcn_raw_ptr_buffer_load_lds(rg, (LAS void*)(dst + 16384 + i * 1024), 16, vw[i >> 1], sw + i * 4 * (FF * 2), 0, 0);
                __builtin_amdgcn_raw_ptr_buffer_load_lds(ru, (LAS void*)(dst + 32768 + i * 1024), 16, vw[i >> 1], sw + i * 4 * (FF * 2), 0, 0);
                __builtin_amdgcn_raw_ptr_buffer_load_lds(rx, (LAS void*)(dst + i * 1024), 16, vx[i & 1], sx + (i >> 1) * 16 * 2048, 0, 0);
            }
        };
        TS(1);
        stage(0); TS(2); stage(1);
        TS(3);
        wait_vm<12>();
        TS(4);
        barrier();
        stage(2);
        wait_vm<12>();
        barrier();
        for (int s = 3; s < G1_NK; ++s) {
            stage(s);
            wait_vm<12>();
            barrier();
        }
        wait_vm<0>();
        barrier();
        TSFLUSH(0, 5);
    } else {
        const int wm = wid >> 1, wn = wid & 1;
        const int fr = lane & 15, g = lane >> 4, q = (lane & 15) >> 2, pp = lane & 3;
        const unsigned lbase = (unsigned)(size_t)smem;
        unsigned ax[2], aw[4];
#pragma unroll
        for (int ks = 0; ks < 2; ++ks) ax[ks] = lbase + (wm * 64 + fr) * 128 + ((((ks * 4 + g) ^ (fr >> 1)) & 7) << 4);
        const int fsw = q | ((g & 1) << 2);
#pragma unroll
        for (int nt = 0; nt < 4; ++nt) aw[nt] = lbase + 16384 + (8 * g + q) * 256 + (((wn * 4 + nt) ^ fsw) << 5) + pp * 8;
        f32x4 acc[4][8];
#pragma unroll
        for (int i = 0; i < 4; ++i)
#pragma unroll
            for (int j = 0; j < 8; ++j) acc[i][j] = (f32x4){0.f, 0.f, 0.f, 0.f};
        bf16x8 xa[4], wa[8], xb[4], wb[8];
        auto rd = [&](bf16x8(&xf)[4], bf16x8(&wf)[8], int ks) {
#pragma unroll
            for (int nt = 0; nt < 8; ++nt) {
                unsigned pw = aw[nt & 3] + (nt >> 2) * 16384 + ks * 8192;
                wf[nt] = lds_tr2(pw, pw + 1024);
                if (nt < 4) xf[nt] = lds_b128(ax[ks] + nt * 2048);
            }
        };
        auto mm = [&](bf16x8(&xf)[4], bf16x8(&wf)[8]) {
#pragma unroll
            for (int nt = 0; nt < 8; ++nt)
#pragma unroll
                for (int mt = 0; mt < 4; ++mt) acc[mt][nt] = __builtin_amdgcn_mfma_f32_16x16x32_bf16(wf[nt], xf[mt], acc[mt][nt], 0, 0, 0);
        };
        SB();
        barrier();
        TS(1);
        rd(xa, wa, 0);
        wait_lgkm0();
        int slot = 0;
        for (int s = 0; s < G1_NK - 1; ++s) {
            rd(xb, wb, 1);
            mm(xa, wa);
            const int d = slot == 2 ? -2 * G1_STAGE : G1_STAGE;
            slot = slot == 2 ? 0 : slot + 1;
#pragma unroll
            for (int i = 0; i < 2; ++i) ax[i] += d;
#pragma unroll
            for (int i = 0; i < 4; ++i) aw[i] += d;
#pragma unroll
            for (int i = 0; i < 20; ++i) { SGB(0x8, 1); SGB(0x100, 1); }
#pragma unroll
            for (int i = 0; i < 6; ++i) { SGB(0x8, 1); SGB(0x2, 1); }
            SGB(0x8, 6);
            SB();
            wait_lgkm0();
            barrier();
            SB();
            rd(xa, wa, 0);
            mm(xb, wb);
#pragma unroll
            for (int i = 0; i < 20; ++i) { SGB(0x8, 1); SGB(0x100, 1); }
            SGB(0x8, 12);
            SB();
            wait_lgkm0();
            SB();
        }
        rd(xb, wb, 1);
        mm(xa, wa);
#pragma unroll
        for (int i = 0; i < 20; ++i) { SGB(0x8, 1); SGB(0x100, 1); }
        SGB(0x8, 12);
        SB();
        mm(xb, wb);
        TS(2);
        char* hp = h + ((size_t)(mb * 56 + fb * 2 + wn) * 16 + wm * 8) * 1024 + lane * 16;
#pragma unroll
        for (int mt = 0; mt < 4; ++mt)
#pragma unroll
            for (int s2 = 0; s2 < 2; ++s2) {
                f32x4 g0 = acc[mt][2 * s2], g1 = acc[mt][2 * s2 + 1], u0 = acc[mt][4 + 2 * s2], u1 = acc[mt][5 + 2 * s2];
                u32x4 o;
                o[0] = pack_bf16(swiglu(g0[0], u0[0]), swiglu(g0[1], u0[1]));
                o[1] = pack_bf16(swiglu(g0[2], u0[2]), swiglu(g0[3], u0[3]));
                o[2] = pack_bf16(swiglu(g1[0], u1[0]), swiglu(g1[1], u1[1]));
                o[3] = pack_bf16(swiglu(g1[2], u1[2]), swiglu(g1[3], u1[3]));
                *(u32x4*)(hp + (mt * 2 + s2) * 1024) = o;
            }
        TS(3);
        wait_vm<0>();
        TS(4);
        TSFLUSH(0, 5);
    }
}

constexpr int G2_STAGE = 32 * 1024, G2_NK = 14, G2_NST = 5;

__global__ __launch_bounds__(512, 2) void ffn_g2(const char* __restrict__ h, const char* __restrict__ wd,
                                                 char* __restrict__ out, char* __restrict__ part,
                                                 unsigned* __restrict__ cnt) {
    extern __shared__ __attribute__((aligned(16))) char smem_[];
    LAS char* smem = (LAS char*)smem_;
    const int tid = threadIdx.x, lane = tid & 63;
    const int wid = __builtin_amdgcn_readfirstlane(tid >> 6);
    TSDECL; TS(0);
    const int b = blockIdx.x;
    const int xcd = b & 7, idx = b >> 3;
    const int mb = idx & 7, nb = (xcd & 1) * 4 + (idx >> 3), kq = xcd >> 1;
#define ROT2 3
    const int rot = ROT2 == 0 ? 0 : ROT2 == 1 ? (mb & 1) * 7 : ROT2 == 2 ? (mb * 14 / 8 + (idx >> 3) * 14 / 4) % 14 : ROT2 == 3 ? mb * 14 / 8 : (mb & 3) * 14 / 4;
    const int tile = mb * 8 + nb;

    if (wid >= 4) {
        const int p = wid - 4;
        auto rh = __builtin_amdgcn_make_buffer_rsrc((void*)h, 0, MTOK * FF * 2, 0x00020000);
        auto rw = __builtin_amdgcn_make_buffer_rsrc((void*)wd, 0, DM * FF * 2, 0x00020000);
        const unsigned vh = lane * 16;
        unsigned vw[2];
#pragma unroll
        for (int e = 0; e < 2; ++e) {
            int f = (lane >> 4) | (e << 2);
            vw[e] = (lane >> 4) * (DM * 2) + (((lane & 15) ^ (f << 1)) << 4);
        }
        const unsigned hbase = (mb * 56 + kq * 14) * 16384 + p * 4096;
        const unsigned wbase = (kq * 896 + p * 16) * (DM * 2) + nb * 256;
        auto stage = [&](int s) {
            int kk = s + rot; if (kk >= G2_NK) kk -= G2_NK;
            LAS char* dst = smem + (s % G2_NST) * G2_STAGE + p * 4096;
            unsigned sh = hbase + kk * 16384;
            unsigned sw = wbase + kk * 64 * (DM * 2);
#pragma unroll
            for (int i = 0; i < 4; ++i) {
                __builtin_amdgcn_raw_ptr_buffer_load_lds(rw, (LAS void*)(dst + 16384 + i * 1024), 16, vw[i & 1], sw + i * 4 * (DM * 2), 0, 0);
                __builtin_amdgcn_raw_ptr_buffer_load_lds(rh, (LAS void*)(dst + i * 1024), 16, vh, sh + i * 1024, 0, 0);
            }
        };
        stage(0); stage(1);
        wait_vm<8>();
        barrier();
        stage(2);
        wait_vm<8>();
        barrier();
        stage(3); stage(4);
        for (int s = 5; s < G2_NK; ++s) {
            stage(s);
            wait_vm<24>();
            barrier();
        }
        wait_vm<16>(); barrier();
        wait_vm<8>(); barrier();
        wait_vm<0>(); barrier();
        barrier();
        barrier();
    } else {
        const int wm = wid >> 1, wn = wid & 1;
        const int g = lane >> 4, q = (lane & 15) >> 2, pp = lane & 3;
        const unsigned lbase = (unsigned)(size_t)smem;
        unsigned ah = lbase + wm * 8192 + lane * 16;
        unsigned aw[4];
        const int fsw = q | ((g & 1) << 2);
#pragma unroll
        for (int nt = 0; nt < 4; ++nt) aw[nt] = lbase + 16384 + (4 * g + q) * 256 + (((wn * 4 + nt) ^ fsw) << 5) + pp * 8;
        f32x4 acc[4][4];
#pragma unroll
        for (int i = 0; i < 4; ++i)
#pragma unroll
            for (int j = 0; j < 4; ++j) acc[i][j] = (f32x4){0.f, 0.f, 0.f, 0.f};
        bf16x8 xa[4], wa[4], xb[4], wb[4];
        auto rd = [&](bf16x8(&xf)[4], bf16x8(&wf)[4], int ks) {
#pragma unroll
            for (int nt = 0; nt < 4; ++nt) {
                unsigned pw = aw[nt] + ks * 8192;
                wf[nt] = lds_tr2(pw, pw + 4096);
                xf[nt] = lds_b128(ah + nt * 2048 + ks * 1024);
            }
        };
        auto mm = [&](bf16x8(&xf)[4], bf16x8(&wf)[4]) {
#pragma unroll
            for (int nt = 0; nt < 4; ++nt)
#pragma unroll
                for (int mt = 0; mt < 4; ++mt) acc[mt][nt] = __builtin_amdgcn_mfma_f32_16x16x32_bf16(wf[nt], xf[mt], acc[mt][nt], 0, 0, 0);
        };
        SB();
        barrier();
        TS(1);
        rd(xa, wa, 0);
        wait_lgkm0();
        int slot = 0;
        for (int s = 0; s < G2_NK - 1; ++s) {
            rd(xb, wb, 1);
            mm(xa, wa);
            const int d = slot == G2_NST - 1 ? -(G2_NST - 1) * G2_STAGE : G2_STAGE;
            slot = slot == G2_NST - 1 ? 0 : slot + 1;
            ah += d;
#pragma unroll
            for (int i = 0; i < 4; ++i) aw[i] += d;
#pragma unroll
            for (int i = 0; i < 12; ++i) { SGB(0x8, 1); SGB(0x100, 1); }
            SGB(0x8, 1); SGB(0x2, 2); SGB(0x8, 1); SGB(0x2, 2); SGB(0x8, 1); SGB(0x2, 1); SGB(0x8, 1);
            SB();
            wait_lgkm0();
            barrier();
            SB();
            rd(xa, wa, 0);
            mm(xb, wb);
#pragma unroll
            for (int i = 0; i < 12; ++i) { SGB(0x8, 1); SGB(0x100, 1); }
            SGB(0x8, 4);
            SB();
            wait_lgkm0();
            SB();
        }
        rd(xb, wb, 1);
        mm(xa, wa);
#pragma unroll
        for (int i = 0; i < 12; ++i) { SGB(0x8, 1); SGB(0x100, 1); }
        SGB(0x8, 4);
        SB();
        mm(xb, wb);
        TS(2);
        auto rp = __builtin_amdgcn_make_buffer_rsrc((void*)part, 0, 64 * 16 * 16384, 0x00020000);
        const unsigned pl = wid * 4096 + lane * 16;
#pragma unroll
        for (int mt = 0; mt < 4; ++mt) {
            if (mt != kq) {
#pragma unroll
                for (int nt = 0; nt < 4; ++nt)
                    __builtin_amdgcn_raw_buffer_store_b128(__builtin_bit_cast(u32x4, acc[mt][nt]), rp, pl + nt * 1024,
                                                           ((tile * 4 + kq) * 4 + mt) * 16384, 16);
            }
        }
        wait_vm<0>();
        TS(3);
        barrier();
        if (wid == 0) {
            unsigned* c = cnt + tile * 64;
            if (lane == 0) __hip_atomic_fetch_add(c, 1u, __ATOMIC_RELAXED, __HIP_MEMORY_SCOPE_AGENT);
            unsigned spins = 0;
            while (__builtin_amdgcn_readfirstlane(__hip_atomic_load(c, __ATOMIC_RELAXED, __HIP_MEMORY_SCOPE_AGENT)) < 4u) {
                __builtin_amdgcn_s_sleep(1);
                if (++spins > 4000000u) break;
            }
        }
        barrier();
        TS(4);
        f32x4 r[4];
#pragma unroll
        for (int mt = 0; mt < 4; ++mt)
            if (mt == kq) {
#pragma unroll
                for (int nt = 0; nt < 4; ++nt) r[nt] = acc[mt][nt];
            }
        u32x4 v[3][4];
#pragma unroll
        for (int j = 0; j < 3; ++j) {
            int src = (kq + 1 + j) & 3;
#pragma unroll
            for (int nt = 0; nt < 4; ++nt)
                v[j][nt] = __builtin_amdgcn_raw_buffer_load_b128(rp, pl + nt * 1024, ((tile * 4 + src) * 4 + kq) * 16384, 16);
        }
#pragma unroll
        for (int j = 0; j < 3; ++j)
#pragma unroll
            for (int nt = 0; nt < 4; ++nt) r[nt] += __builtin_bit_cast(f32x4, v[j][nt]);
        char* op = out + (size_t)(mb * 128 + wm * 64 + kq * 16 + (lane & 15)) * (DM * 2) + (nb * 128 + wn * 64 + g * 4) * 2;
#pragma unroll
        for (int nt = 0; nt < 4; ++nt) {
            u32x2 o = {pack_bf16(r[nt][0], r[nt][1]), pack_bf16(r[nt][2], r[nt][3])};
            *(u32x2*)(op + nt * 32) = o;
        }
        TS(5);
        wait_vm<0>();
        TS(6);
        TSFLUSH(1 << 20, 7);
    }
}

extern "C" void kernel_launch(void* const* d_in, const int* in_sizes, int n_in, void* d_out, int out_size, void* d_ws,
                              size_t ws_size, hipStream_t stream) {
    const char* x = (const char*)d_in[0];
    const char* wg = (const char*)d_in[1];
    const char* wu = (const char*)d_in[2];
    const char* wd = (const char*)d_in[3];
    char* ws = (char*)d_ws;
    unsigned* cnt = (unsigned*)ws;
    char* h = ws + 65536;
    char* part = ws + 65536 + (8u << 20);
    (void)hipFuncSetAttribute((const void*)ffn_g1, hipFuncAttributeMaxDynamicSharedMemorySize, 3 * G1_STAGE);
    (void)hipFuncSetAttribute((const void*)ffn_g2, hipFuncAttributeMaxDynamicSharedMemorySize, G2_NST * G2_STAGE);
    ffn_g1<<<224, 512, 3 * G1_STAGE, stream>>>(x, wg, wu, h, cnt);
    ffn_g2<<<256, 512, G2_NST * G2_STAGE, stream>>>(h, wd, (char*)d_out, part, cnt);
}
```

```cpp
#include <hip/hip_runtime.h>
#include <hip/hip_bf16.h>

typedef short s16x4 __attribute__((ext_vector_type(4)));
typedef __bf16 bf16x8 __attribute__((ext_vector_type(8)));
typedef __bf16 bf16x2 __attribute__((ext_vector_type(2)));
typedef float f32x4 __attribute__((ext_vector_type(4)));
typedef unsigned u32x4 __attribute__((ext_vector_type(4)));
typedef unsigned u32x2 __attribute__((ext_vector_type(2)));
typedef unsigned long long u64;
typedef __amdgpu_buffer_rsrc_t rsrc_t;
#define LAS __attribute__((address_space(3)))

constexpr int MTOK = 1024, DM = 1024, FF = 3584;
constexpr int LDS_BYTES = 160 * 1024;
constexpr unsigned WS_EPOCH = 0;
constexpr unsigned WS_HFLAG = 4096;
constexpr unsigned WS_PFLAG = 16384;
constexpr unsigned WS_H = 65536;
constexpr unsigned WS_PART = 65536 + (8u << 20);
constexpr unsigned WS_BYTES = WS_PART + 64 * 4 * 3 * 16384;
constexpr unsigned SPIN_CAP = 2000000u;

template <int N> __device__ __forceinline__ void wait_vm() { asm volatile("s_waitcnt vmcnt(%0)" ::"i"(N) : "memory"); }
__device__ __forceinline__ void wait_lgkm0() { __builtin_amdgcn_s_waitcnt(0xC07F); }
__device__ __forceinline__ void barrier() {
    asm volatile("" ::: "memory");
    __builtin_amdgcn_s_barrier();
    asm volatile("" ::: "memory");
}
__device__ __forceinline__ void dma(rsrc_t r, unsigned lds, unsigned voff, unsigned soff) {
    asm volatile("s_mov_b32 m0, %0\n\ts_nop 0\n\tbuffer_load_dwordx4 %1, %2, %3 offen lds" ::"s"(lds), "v"(voff), "s"(r), "s"(soff) : "memory");
}
__device__ __forceinline__ void dma2(rsrc_t r, unsigned lds, unsigned v0, unsigned v1, unsigned soff) {
    asm volatile("s_mov_b32 m0, %0\n\ts_nop 0\n\t"
                 "buffer_load_dwordx4 %1, %3, %4 offen lds\n\t"
                 "buffer_load_dwordx4 %2, %3, %4 offen offset:1024 lds"
                 ::"s"(lds), "v"(v0), "v"(v1), "s"(r), "s"(soff) : "memory");
}
__device__ __forceinline__ void dma4(rsrc_t r, unsigned lds, const unsigned (&v)[4], unsigned soff) {
    asm volatile("s_mov_b32 m0, %0\n\ts_nop 0\n\t"
                 "buffer_load_dwordx4 %1, %5, %6 offen lds\n\t"
                 "buffer_load_dwordx4 %2, %5, %6 offen offset:1024 lds\n\t"
                 "buffer_load_dwordx4 %3, %5, %6 offen offset:2048 lds\n\t"
                 "buffer_load_dwordx4 %4, %5, %6 offen offset:3072 lds"
                 ::"s"(lds), "v"(v[0]), "v"(v[1]), "v"(v[2]), "v"(v[3]), "s"(r), "s"(soff) : "memory");
}
__device__ __forceinline__ void dma7_sc1(rsrc_t r, unsigned lds, unsigned voff, unsigned soff) {
    asm volatile("s_mov_b32 m0, %0\n\ts_nop 0\n\t"
                 "buffer_load_dwordx4 %1, %2, %3 offen sc1 lds\n\t"
                 "buffer_load_dwordx4 %1, %2, %3 offen offset:1024 sc1 lds\n\t"
                 "buffer_load_dwordx4 %1, %2, %3 offen offset:2048 sc1 lds\n\t"
                 "buffer_load_dwordx4 %1, %2, %3 offen offset:3072 sc1 lds\n\t"
                 "s_mov_b32 m0, %4\n\ts_nop 0\n\t"
                 "buffer_load_dwordx4 %1, %2, %5 offen sc1 lds\n\t"
                 "buffer_load_dwordx4 %1, %2, %5 offen offset:1024 sc1 lds\n\t"
                 "buffer_load_dwordx4 %1, %2, %5 offen offset:2048 sc1 lds"
                 ::"s"(lds), "v"(voff), "s"(r), "s"(soff), "s"(lds + 4096), "s"(soff + 4096) : "memory");
}
__device__ __forceinline__ bf16x8 lds_b128(unsigned p) { return *(LAS bf16x8*)(size_t)p; }
__device__ __forceinline__ bf16x8 lds_tr2(unsigned p0, unsigned p1) {
    s16x4 a = __builtin_amdgcn_ds_read_tr16_b64_v4i16((LAS s16x4*)(size_t)p0);
    s16x4 b = __builtin_amdgcn_ds_read_tr16_b64_v4i16((LAS s16x4*)(size_t)p1);
    return __builtin_bit_cast(bf16x8, __builtin_shufflevector(a, b, 0, 1, 2, 3, 4, 5, 6, 7));
}
__device__ __forceinline__ unsigned pack_bf16(float a, float b) {
    bf16x2 v = {(__bf16)a, (__bf16)b};
    return __builtin_bit_cast(unsigned, v);
}
__device__ __forceinline__ float swiglu(float g, float u) {
    float e = __builtin_amdgcn_exp2f(g * -1.44269504088896341f);
    return g * u * __builtin_amdgcn_rcpf(1.0f + e);
}
__device__ __forceinline__ u64 mix64(u64 z) {
    z += 0x9e3779b97f4a7c15ull;
    z = (z ^ (z >> 30)) * 0xbf58476d1ce4e5b9ull;
    z = (z ^ (z >> 27)) * 0x94d049bb133111ebull;
    return z ^ (z >> 31);
}
__device__ __forceinline__ u64 sload64(const char* p) {
    u64 v;
    asm volatile("s_load_dwordx2 %0, %1, 0x0\n\ts_waitcnt lgkmcnt(0)" : "=&s"(v) : "s"(p) : "memory");
    return v;
}
__device__ __forceinline__ void flag_store(rsrc_t r, unsigned soff, u64 tag) {
    u32x2 v = {(unsigned)tag, (unsigned)(tag >> 32)};
    __builtin_amdgcn_raw_buffer_store_b64(v, r, 0, soff, 16);
}
__device__ __forceinline__ void flag_wait(rsrc_t r, unsigned soff, int n, u64 tag, int lane) {
    const unsigned vo = (lane < n ? lane : 0) * 8;
#pragma nounroll
    for (unsigned spins = 0; spins < SPIN_CAP; ++spins) {
        u32x2 v = __builtin_amdgcn_raw_buffer_load_b64(r, vo, soff, 16);
        bool ok = v[0] == (unsigned)tag && v[1] == (unsigned)(tag >> 32);
        if (__all(ok)) break;
        __builtin_amdgcn_s_sleep(1);
        asm volatile("" ::: "memory");
    }
    asm volatile("" ::: "memory");
}
#define TSDECL
#define TS(i)
#define TSFLUSH(n)
#define DIAG(...)
#define SGB(mask, n) __builtin_amdgcn_sched_group_barrier(mask, n, 0)
#define SB() __builtin_amdgcn_sched_barrier(0)
#define SETPRIO(n) __builtin_amdgcn_s_setprio(n)
#define SPIN_SLEEP 1
__device__ __forceinline__ void mfma_acc(f32x4& c, const bf16x8& a, const bf16x8& b) {
    asm volatile("v_mfma_f32_16x16x32_bf16 %0, %1, %2, %0" : "+v"(c) : "v"(a), "v"(b));
}
__device__ __forceinline__ void mfma_new(f32x4& c, const bf16x8& a, const bf16x8& b) {
    asm volatile("v_mfma_f32_16x16x32_bf16 %0, %1, %2, 0" : "=&v"(c) : "v"(a), "v"(b));
}
__device__ __forceinline__ s16x4 lds_tr(unsigned p) { return __builtin_amdgcn_ds_read_tr16_b64_v4i16((LAS s16x4*)(size_t)p); }
__device__ __forceinline__ bf16x8 cat8(s16x4 a, s16x4 b) { return __builtin_bit_cast(bf16x8, __builtin_shufflevector(a, b, 0, 1, 2, 3, 4, 5, 6, 7)); }

template <int V> struct IC { static constexpr int value = V; };
template <int I, int N, class F> __device__ __forceinline__ void static_for(F&& f) {
    if constexpr (I < N) { f(IC<I>{}); static_for<I + 1, N>(f); }
}
constexpr int G1_STAGE = 48 * 1024, G1_NK = 16;
constexpr int RING = 14336;
constexpr int L_WRES = 88 * 1024, L_WST = 144 * 1024;
__host__ __device__ constexpr bool isL(int i) { return i % 4 == 2; }
__host__ __device__ constexpr int Lidx(int i) { return i / 4; }
__host__ __device__ constexpr int Ridx(int i) { return i - i / 4 - (i % 4 == 3 ? 1 : 0); }
__host__ __device__ constexpr int Rstep(int n) { return 4 * (n / 3) + (n % 3 == 2 ? 3 : n % 3); }
__host__ __device__ constexpr int Lstep(int l) { return 4 * l + 2; }

#define ROT1 5
#define PF_DELAY 64
#define ST_SPREAD 1
#define FLAG_AT 20
#define WS0 5
__host__ __device__ constexpr int wfirst(int s) { return s <= WS0 ? 0 : (21 * (s - WS0)) / (16 - WS0); }
__host__ __device__ constexpr int wcnt(int s) { return s < WS0 ? 0 : wfirst(s + 1) - wfirst(s); }
static_assert(wfirst(16) == 21 && WS0 >= 2 && WS0 <= 5, "Wd schedule");

__global__ __launch_bounds__(512, 2) void ffn_fused(const char* __restrict__ x, const char* __restrict__ wg,
                                                    const char* __restrict__ wu, const char* __restrict__ wd,
                                                    char* __restrict__ out, char* __restrict__ ws) {
    extern __shared__ __attribute__((aligned(16))) char smem_[];
    const unsigned lbase = (unsigned)(size_t)(LAS char*)smem_;
    const int tid = threadIdx.x, lane = tid & 63;
    const int wid = __builtin_amdgcn_readfirstlane(tid >> 6);
    TSDECL; TS(0);
    asm volatile("" ::"s"(x), "s"(wg), "s"(wu), "s"(wd), "s"(out), "s"(ws));
    const int b = blockIdx.x;
    const int xcd = b & 7, idx = b >> 3;
    const int mb = idx & 7, j = idx >> 3;
    const int kq = xcd >> 1, hi = xcd & 1, nb = hi * 4 + j;
    const bool helper = (j == 3) && ((mb >> 2) != hi);
    const int fb = 7 * kq + (j == 3 ? 3 : nb);
    const int tile = mb * 8 + nb;
#define OWNR 2
    const int own = (kq + (OWNR == 0 ? nb : OWNR == 1 ? 2 * nb : 0)) & 3;
    const rsrc_t rws = __builtin_amdgcn_make_buffer_rsrc((void*)ws, 0, WS_BYTES, 0x00020000);
    const rsrc_t rd_ = __builtin_amdgcn_make_buffer_rsrc((void*)wd, 0, DM * FF * 2, 0x00020000);
    const int g = lane >> 4, q = (lane & 15) >> 2, pp = lane & 3;

    if (wid >= 4) {
        const int p = wid - 4;
        const unsigned vwd0 = (lane >> 2) * (DM * 2) + (((lane & 3) ^ (((lane >> 4) & 1) << 1)) << 4);
        const unsigned vwd1 = vwd0 + 16 * (DM * 2) - 1024;
        const unsigned wdbase = kq * 896 * (DM * 2) + nb * 256 + p * 64;
        auto wd_step = [&](int i, unsigned dst) { dma2(rd_, dst, vwd0, vwd1, wdbase + i * 32 * (DM * 2)); };
        const unsigned wst = lbase + L_WST + p * 4096, wres = lbase + L_WRES + p * 14336;
        unsigned at[2];
#pragma unroll
        for (int nt = 0; nt < 2; ++nt) at[nt] = (4 * g + q) * 64 + ((nt ^ (g & 1)) << 5) + pp * 8;
        bf16x8 wr[21][2];
        auto wd_read = [&](int n, int cnt) {
#pragma unroll
            for (int e = 0; e < cnt; ++e)
#pragma unroll
                for (int nt = 0; nt < 2; ++nt) wr[n + e][nt] = lds_tr2(wst + at[nt] + e * 2048, wst + at[nt] + e * 2048 + 1024);
            SB();
        };
        auto wd_done = [&](int n, int cnt) {
            asm volatile("s_waitcnt lgkmcnt(0)" : "+v"(wr[n][0]), "+v"(wr[n][1])::"memory");
            if (cnt > 1) asm volatile("" : "+v"(wr[n + 1][0]), "+v"(wr[n + 1][1])::"memory");
        };
        auto wd_issue = [&](int n, int cnt) { wd_step(Rstep(n), wst); if (cnt > 1) wd_step(Rstep(n + 1), wst + 2048); };
        u64 tag;
        if (!helper) {
            const rsrc_t rx = __builtin_amdgcn_make_buffer_rsrc((void*)x, 0, MTOK * DM * 2, 0x00020000);
            const rsrc_t rg = __builtin_amdgcn_make_buffer_rsrc((void*)wg, 0, DM * FF * 2, 0x00020000);
            const rsrc_t ru = __builtin_amdgcn_make_buffer_rsrc((void*)wu, 0, DM * FF * 2, 0x00020000);
            const int rot = ROT1 == 0 ? 0 : ROT1 == 2 ? (mb & 3) * 4 : ROT1 == 4 ? 2 * mb : ROT1 == 5 ? (2 * (mb + xcd)) & 15 : ROT1 == 6 ? (4 * (mb + xcd)) & 15 : (8 * (mb + xcd)) & 15;
            unsigned vx[4], vw[4];
#pragma unroll
            for (int i = 0; i < 4; ++i) {
                const int r = 8 * i + (lane >> 3);
                vx[i] = r * 2048 + (((lane & 7) ^ ((r >> 1) & 7)) << 4) - i * 1024;
                const int f = (lane >> 4) | ((i >> 1) << 2);
                vw[i] = (4 * i + (lane >> 4)) * (FF * 2) + (((lane & 15) ^ (f << 1)) << 4) - i * 1024;
            }
            const unsigned xbase = (mb * 128 + p * 32) * 2048;
            const unsigned wbase = (p * 16) * (FF * 2) + fb * 256;
            auto stage_w = [&](int s) {
                const unsigned dst = lbase + (s % 3) * G1_STAGE + p * 4096, so = wbase + ((s + rot) & 15) * 64 * (FF * 2);
                dma4(rg, dst + 16384, vw, so);
                dma4(ru, dst + 32768, vw, so);
            };
            auto stage_x = [&](int s) { dma4(rx, lbase + (s % 3) * G1_STAGE + p * 4096, vx, xbase + ((s + rot) & 15) * 128); };
            stage_w(0); stage_x(0); stage_w(1); stage_x(1);
            tag = mix64(sload64(ws + WS_EPOCH + mb * 256));
            asm volatile("" : "+s"(tag));
            wait_vm<12>();
            barrier();
#pragma unroll
            for (int s = 2; s < G1_NK; ++s) {
                stage_w(s);
                wait_vm<8>();
                if (wcnt(s - 1)) wd_read(wfirst(s - 1), wcnt(s - 1));
                stage_x(s);
                if (wcnt(s - 1)) wd_done(wfirst(s - 1), wcnt(s - 1));
                if (wcnt(s)) wd_issue(wfirst(s), wcnt(s));
                barrier();
            }
            wait_vm<0>();
            barrier();
#pragma unroll
            for (int l = 0; l < 7; ++l) wd_step(Lstep(l), wres + l * 2048);
            if (wcnt(15)) { wd_read(wfirst(15), wcnt(15)); wd_done(wfirst(15), wcnt(15)); }
            wait_vm<0>();
        } else {
            tag = mix64(sload64(ws + WS_EPOCH + mb * 256));
            asm volatile("" : "+s"(tag));
            barrier();
            barrier();
#pragma unroll
            for (int l = 0; l < 7; ++l) wd_step(Lstep(l), wres + l * 2048);
#pragma unroll
            for (int c = 0; c < 11; ++c) { const int n = c < 10 ? 2 : 1; wd_issue(2 * c, n); wait_vm<0>(); wd_read(2 * c, n); wd_done(2 * c, n); }
        }
        TS(1); TS(2);
        const unsigned awl[2] = {wres + at[0], wres + at[1]};
        unsigned hl[2] = {lbase + lane * 16, lbase + lane * 16 + 3 * RING};
        asm volatile("" : "+v"(hl[1]));
        auto hread = [&](int gs, int rt) {
            const int slot = (gs / 7) % 6;
            return lds_b128(hl[slot / 3] + (slot % 3) * RING + rt * 7168 + (gs % 7) * 1024);
        };
        bf16x8 hf[4][2];
        s16x4 wt[2][2];
        f32x4 acc[2][4];
        const unsigned pso = WS_PART + (tile * 4 * 3 * 4 + p) * 4096, pfo = WS_PFLAG + tile * 512 + p * 8;
        wt[0][0] = lds_tr(awl[0]); wt[0][1] = lds_tr(awl[0] + 1024);
        barrier();
        barrier();
        TS(3);
#pragma unroll
        for (int i = 0; i < 3; ++i) { hf[i][0] = hread(i, 0); hf[i][1] = hread(i, 1); }
        static_for<0, 112>([&](auto GS) __attribute__((always_inline)) {
            constexpr int gs = decltype(GS)::value;
            constexpr int qi = gs / 28, i = gs % 28, nx = gs + 3;
            constexpr int l3 = (i + 3) % 28, l2 = (i + 2) % 28;
            constexpr int lf = isL(l3) ? l3 : isL(l2) ? l2 : -1, lnt = isL(l3) ? 0 : 1;
            constexpr bool more = nx < 112;
            const bf16x8 w0 = isL(i) ? cat8(wt[0][0], wt[0][1]) : wr[isL(i) ? 0 : Ridx(i)][0];
            const bf16x8 w1 = isL(i) ? cat8(wt[1][0], wt[1][1]) : wr[isL(i) ? 0 : Ridx(i)][1];
            f32x4(&c)[4] = acc[qi & 1];
            bf16x8 hn0, hn1;
            SB();
            if (i == 0) mfma_new(c[2], w0, hf[gs % 4][1]); else mfma_acc(c[2], w0, hf[gs % 4][1]);
            SB();
            if (more && nx % 7 == 0) barrier();
            if (more) hn0 = hread(nx, 0);
            SB();
            if (i == 0) mfma_new(c[0], w0, hf[gs % 4][0]); else mfma_acc(c[0], w0, hf[gs % 4][0]);
            SB();
            if (more) hn1 = hread(nx, 1);
            SB();
            if (i == 0) mfma_new(c[3], w1, hf[gs % 4][1]); else mfma_acc(c[3], w1, hf[gs % 4][1]);
            SB();
            if (lf >= 0 && gs < 110) wt[lnt][0] = lds_tr(awl[lnt] + Lidx(lf) * 2048);
            SB();
            if (i == 0) mfma_new(c[1], w1, hf[gs % 4][0]); else mfma_acc(c[1], w1, hf[gs % 4][0]);
            SB();
            if (lf >= 0 && gs < 110) wt[lnt][1] = lds_tr(awl[lnt] + Lidx(lf) * 2048 + 1024);
            if (more) { hf[nx % 4][0] = hn0; hf[nx % 4][1] = hn1; }
            if (qi > 0 && i < 4 * ST_SPREAD && (ST_SPREAD == 1 || p == i % ST_SPREAD))
                __builtin_amdgcn_raw_buffer_store_b128(__builtin_bit_cast(u32x4, acc[(qi - 1) & 1][(i / ST_SPREAD) & 3]), rws, lane * 16 + (i / ST_SPREAD) * 1024,
                                                       pso + (((kq + qi) & 3) * 3 + qi - 1) * 16384, 16);
            if (qi > 0 && i == FLAG_AT) {
                wait_vm<0>();
                flag_store(rws, pfo + ((kq + qi) & 3) * 128 + (qi - 1) * 32, tag);
            }
            SB();
        });
        asm volatile("s_nop 7\n\ts_nop 1" : "+v"(acc[1][0]), "+v"(acc[1][1]), "+v"(acc[1][2]), "+v"(acc[1][3]));
#pragma unroll
        for (int e = 0; e < 4; ++e) *(LAS f32x4*)(size_t)(wst + e * 1024 + lane * 16) = acc[1][e];
        wait_lgkm0();
        TS(4);
        barrier();
        TSFLUSH(5);
    } else {
        u64 tag = mix64(sload64(ws + WS_EPOCH + mb * 256));
        asm volatile("" : "+s"(tag));
        const unsigned myflag = WS_HFLAG + ((mb * 4 + kq) * 8 + nb) * 32 + wid * 8;
        if (!helper) {
            const int wm = wid >> 1, wn = wid & 1;
            const int fr = lane & 15;
            unsigned ax[2], aw[4];
#pragma unroll
            for (int ks = 0; ks < 2; ++ks) ax[ks] = lbase + (wm * 64 + fr) * 128 + ((((ks * 4 + g) ^ (fr >> 1)) & 7) << 4);
            const int fsw = q | ((g & 1) << 2);
#pragma unroll
            for (int nt = 0; nt < 4; ++nt) aw[nt] = lbase + 16384 + (8 * g + q) * 256 + (((wn * 4 + nt) ^ fsw) << 5) + pp * 8;
            f32x4 acc[4][8];
#pragma unroll
            for (int i = 0; i < 4; ++i)
#pragma unroll
                for (int jj = 0; jj < 8; ++jj) { acc[i][jj] = (f32x4){0.f, 0.f, 0.f, 0.f}; asm volatile("" : "+v"(acc[i][jj])); }
            bf16x8 xa[4], wa[8], xb[4], wb[8];
            auto rd = [&](bf16x8(&xf)[4], bf16x8(&wf)[8], int ks) {
#pragma unroll
                for (int nt = 0; nt < 8; ++nt) {
                    unsigned pw = aw[nt & 3] + (nt >> 2) * 16384 + ks * 8192;
                    wf[nt] = lds_tr2(pw, pw + 1024);
                    if (nt < 4) xf[nt] = lds_b128(ax[ks] + nt * 2048);
                }
            };
            auto mm = [&](bf16x8(&xf)[4], bf16x8(&wf)[8]) {
#pragma unroll
                for (int nt = 0; nt < 8; ++nt)
#pragma unroll
                    for (int mt = 0; mt < 4; ++mt) acc[mt][nt] = __builtin_amdgcn_mfma_f32_16x16x32_bf16(wf[nt], xf[mt], acc[mt][nt], 0, 0, 0);
            };
            SB();
            barrier();
            TS(1);
            rd(xa, wa, 0);
            wait_lgkm0();
            int slot = 0;
            for (int s = 0; s < G1_NK - 1; ++s) {
                rd(xb, wb, 1);
                mm(xa, wa);
                const int d = slot == 2 ? -2 * G1_STAGE : G1_STAGE;
                slot = slot == 2 ? 0 : slot + 1;
#pragma unroll
                for (int i = 0; i < 2; ++i) ax[i] += d;
#pragma unroll
                for (int i = 0; i < 4; ++i) aw[i] += d;
#pragma unroll
                for (int i = 0; i < 20; ++i) { SGB(0x8, 1); SGB(0x100, 1); }
#pragma unroll
                for (int i = 0; i < 6; ++i) { SGB(0x8, 1); SGB(0x2, 1); }
                SGB(0x8, 6);
                SB();
                wait_lgkm0();
                barrier();
                SB();
                rd(xa, wa, 0);
                mm(xb, wb);
#pragma unroll
                for (int i = 0; i < 20; ++i) { SGB(0x8, 1); SGB(0x100, 1); }
                SGB(0x8, 12);
                SB();
                wait_lgkm0();
                SB();
            }
            rd(xb, wb, 1);
            mm(xa, wa);
#pragma unroll
            for (int i = 0; i < 20; ++i) { SGB(0x8, 1); SGB(0x100, 1); }
            SGB(0x8, 12);
            SB();
            mm(xb, wb);
            TS(2);
            const unsigned hso = WS_H + ((mb * 8 + wm * 4) * 112 + fb * 4 + wn * 2) * 1024;
#pragma unroll
            for (int mt = 0; mt < 4; ++mt)
#pragma unroll
                for (int s2 = 0; s2 < 2; ++s2) {
                    f32x4 g0 = acc[mt][2 * s2], g1 = acc[mt][2 * s2 + 1], u0 = acc[mt][4 + 2 * s2], u1 = acc[mt][5 + 2 * s2];
                    u32x4 o;
                    o[0] = pack_bf16(swiglu(g0[0], u0[0]), swiglu(g0[1], u0[1]));
                    o[1] = pack_bf16(swiglu(g0[2], u0[2]), swiglu(g0[3], u0[3]));
                    o[2] = pack_bf16(swiglu(g1[0], u1[0]), swiglu(g1[1], u1[1]));
                    o[3] = pack_bf16(swiglu(g1[2], u1[2]), swiglu(g1[3], u1[3]));
                    __builtin_amdgcn_raw_buffer_store_b128(o, rws, lane * 16 + s2 * 1024, hso + mt * 112 * 1024, 16);
                }
            TS(3);
            wait_vm<0>();
            flag_store(rws, myflag, tag);
        } else {
            barrier();
            flag_store(rws, myflag, tag);
            TS(1);
            __builtin_amdgcn_s_sleep(PF_DELAY);
            const unsigned vt = (lane >> 3) * (DM * 2) + (lane & 7) * 128;
            const unsigned st = (kq * 896 + ((mb & 3) * 4 + wid) * 8) * (DM * 2) + hi * 1024;
            unsigned sink;
#pragma unroll
            for (int e = 0; e < 7; ++e)
                asm volatile("buffer_load_dword %0, %1, %2, %3 offen" : "=v"(sink) : "v"(vt), "s"(rd_), "s"(st + e * 128 * (DM * 2)) : "memory");
            wait_vm<0>();
            TS(2);
            barrier();
            TS(3);
        }
        TS(4);
        barrier();
        TS(5);
        if (wid < 2) {
            flag_wait(rws, WS_HFLAG + (mb * 4 + kq) * 256, 32, tag, lane);
            TS(6);
            const unsigned vh = lane * 16;
            auto hstage = [&](int t, int slot) {
                const int qq = (own + 1 + (t >> 2)) & 3;
                const unsigned rowb = WS_H + ((mb * 8 + 2 * qq + wid) * 112 + kq * 28) * 1024;
                dma7_sc1(rws, lbase + slot * RING + wid * 7168, vh, rowb + (t & 3) * 7168);
            };
            hstage(0, 0); hstage(1, 1);
            wait_vm<7>();
            barrier();
            TS(7);
            hstage(2, 2); hstage(3, 3);
            int slot = 4;
            for (int t = 4; t < 16; ++t) {
                hstage(t, slot);
                slot = slot == 5 ? 0 : slot + 1;
                wait_vm<21>();
                barrier();
            }
            wait_vm<14>(); barrier();
            wait_vm<7>(); barrier();
            wait_vm<0>(); barrier();
            TS(8);
            barrier();
            TSFLUSH(9);
        } else {
            const int pw0 = 2 * (wid - 2);
            const unsigned pf = WS_PFLAG + (tile * 4 + kq) * 128 + pw0 * 8;
            const unsigned pd = WS_PART + ((tile * 4 + kq) * 3 * 4 + pw0) * 4096;
            u32x4 v[3][8];
            for (int t = 0; t < 13; ++t) barrier();
            flag_wait(rws, pf, 2, tag, lane);
#pragma unroll
            for (int e = 0; e < 8; ++e) v[0][e] = __builtin_amdgcn_raw_buffer_load_b128(rws, lane * 16 + (e & 3) * 1024, pd + (e >> 2) * 4096, 16);
            for (int t = 13; t < 16; ++t) barrier();
            flag_wait(rws, pf + 32, 2, tag, lane);
#pragma unroll
            for (int e = 0; e < 8; ++e) v[1][e] = __builtin_amdgcn_raw_buffer_load_b128(rws, lane * 16 + (e & 3) * 1024, pd + 16384 + (e >> 2) * 4096, 16);
            TS(6);
            flag_wait(rws, pf + 64, 2, tag, lane);
            TS(7);
#pragma unroll
            for (int e = 0; e < 8; ++e) v[2][e] = __builtin_amdgcn_raw_buffer_load_b128(rws, lane * 16 + (e & 3) * 1024, pd + 32768 + (e >> 2) * 4096, 16);
            barrier();
            TS(8);
            char* op = out + (size_t)(mb * 128 + own * 32 + (lane & 15)) * (DM * 2) + (nb * 128 + pw0 * 32 + g * 4) * 2;
#pragma unroll
            for (int e = 0; e < 8; ++e) {
                f32x4 r = *(LAS f32x4*)(size_t)(lbase + L_WST + pw0 * 4096 + e * 1024 + lane * 16);
                r += __builtin_bit_cast(f32x4, v[0][e]);
                r += __builtin_bit_cast(f32x4, v[1][e]);
                r += __builtin_bit_cast(f32x4, v[2][e]);
                u32x2 o = {pack_bf16(r[0], r[1]), pack_bf16(r[2], r[3])};
                *(u32x2*)(op + (size_t)((e >> 1) & 1) * 16 * (DM * 2) + (e >> 2) * 64 + (e & 1) * 32) = o;
            }
            if (wid == 2 && nb == 0 && kq == 0 && lane == 0) *(u64*)(ws + WS_EPOCH + mb * 256) = tag;
            TS(9);
            TSFLUSH(10);
        }
    }
}

extern "C" void kernel_launch(void* const* d_in, const int* in_sizes, int n_in, void* d_out, int out_size, void* d_ws,
                              size_t ws_size, hipStream_t stream) {
    (void)hipFuncSetAttribute((const void*)ffn_fused, hipFuncAttributeMaxDynamicSharedMemorySize, LDS_BYTES);
    ffn_fused<<<256, 512, LDS_BYTES, stream>>>((const char*)d_in[0], (const char*)d_in[1], (const char*)d_in[2],
                                               (const char*)d_in[3], (char*)d_out, (char*)d_ws);
}
```

```cpp
#include <hip/hip_runtime.h>
#include <hip/hip_bf16.h>

typedef short s16x4 __attribute__((ext_vector_type(4)));
typedef __bf16 bf16x8 __attribute__((ext_vector_type(8)));
typedef __bf16 bf16x2 __attribute__((ext_vector_type(2)));
typedef float f32x4 __attribute__((ext_vector_type(4)));
typedef float f32x2 __attribute__((ext_vector_type(2)));
typedef unsigned u32x4 __attribute__((ext_vector_type(4)));
typedef unsigned u32x2 __attribute__((ext_vector_type(2)));
typedef unsigned long long u64;
typedef __amdgpu_buffer_rsrc_t rsrc_t;
#define LAS __attribute__((address_space(3)))

constexpr int MTOK = 1024, DM = 1024, FF = 3584;
constexpr int LDS_BYTES = 160 * 1024;
constexpr unsigned WS_EPOCH = 0;
constexpr unsigned WS_H = 65536;
constexpr unsigned WS_PART = 8u << 20;
constexpr unsigned PBLK = 2304;
constexpr unsigned WS_HFLAG = 20u << 20;
constexpr unsigned INBOX = 512, INB_B = 256;
constexpr unsigned WS_BYTES = WS_HFLAG + 256 * INBOX;
static_assert(WS_H + 64 * 112 * 1024 <= WS_PART && WS_PART + 64 * 4 * 3 * 4 * PBLK <= WS_HFLAG, "workspace map");
constexpr unsigned SPIN_CAP = 2000000u;

template <int N> __device__ __forceinline__ void wait_vm() { asm volatile("s_waitcnt vmcnt(%0)" ::"i"(N) : "memory"); }
__device__ __forceinline__ void wait_lgkm0() { __builtin_amdgcn_s_waitcnt(0xC07F); }
__device__ __forceinline__ void barrier() {
    asm volatile("" ::: "memory");
    __builtin_amdgcn_s_barrier();
    asm volatile("" ::: "memory");
}
__device__ __forceinline__ void dma2(rsrc_t r, unsigned lds, unsigned v0, unsigned v1, unsigned soff) {
    asm volatile("s_mov_b32 m0, %0\n\ts_nop 0\n\t"
                 "buffer_load_dwordx4 %1, %3, %4 offen lds\n\t"
                 "buffer_load_dwordx4 %2, %3, %4 offen offset:1024 lds"
                 ::"s"(lds), "v"(v0), "v"(v1), "s"(r), "s"(soff) : "memory");
}
__device__ __forceinline__ void dma4(rsrc_t r, unsigned lds, const unsigned (&v)[4], unsigned soff) {
    asm volatile("s_mov_b32 m0, %0\n\ts_nop 0\n\t"
                 "buffer_load_dwordx4 %1, %5, %6 offen lds\n\t"
                 "buffer_load_dwordx4 %2, %5, %6 offen offset:1024 lds\n\t"
                 "buffer_load_dwordx4 %3, %5, %6 offen offset:2048 lds\n\t"
                 "buffer_load_dwordx4 %4, %5, %6 offen offset:3072 lds"
                 ::"s"(lds), "v"(v[0]), "v"(v[1]), "v"(v[2]), "v"(v[3]), "s"(r), "s"(soff) : "memory");
}
__device__ __forceinline__ void dma7_sc1(rsrc_t r, unsigned lds, unsigned voff, unsigned soff) {
    asm volatile("s_mov_b32 m0, %0\n\ts_nop 0\n\t"
                 "buffer_load_dwordx4 %1, %2, %3 offen sc1 lds\n\t"
                 "buffer_load_dwordx4 %1, %2, %3 offen offset:1024 sc1 lds\n\t"
                 "buffer_load_dwordx4 %1, %2, %3 offen offset:2048 sc1 lds\n\t"
                 "buffer_load_dwordx4 %1, %2, %3 offen offset:3072 sc1 lds\n\t"
                 "s_mov_b32 m0, %4\n\ts_nop 0\n\t"
                 "buffer_load_dwordx4 %1, %2, %5 offen sc1 lds\n\t"
                 "buffer_load_dwordx4 %1, %2, %5 offen offset:1024 sc1 lds\n\t"
                 "buffer_load_dwordx4 %1, %2, %5 offen offset:2048 sc1 lds"
                 ::"s"(lds), "v"(voff), "s"(r), "s"(soff), "s"(lds + 4096), "s"(soff + 4096) : "memory");
}
template <int OFF> __device__ __forceinline__ void store16_sc1(u32x4 v, rsrc_t r, unsigned voff, unsigned soff) {
    asm volatile("buffer_store_dwordx4 %0, %1, %2, %3 offen offset:%4 sc1\n\ts_nop 1" ::"v"(v), "v"(voff), "s"(r), "s"(soff), "i"(OFF) : "memory");
}
__device__ __forceinline__ void store16(u32x4 v, rsrc_t r, unsigned voff, unsigned soff) {
    asm volatile("buffer_store_dwordx4 %0, %1, %2, %3 offen\n\ts_nop 1" ::"v"(v), "v"(voff), "s"(r), "s"(soff) : "memory");
}
__device__ __forceinline__ void touch(unsigned& sink, rsrc_t r, unsigned voff, unsigned soff) {
    asm volatile("buffer_load_dword %0, %1, %2, %3 offen" : "+v"(sink) : "v"(voff), "s"(r), "s"(soff) : "memory");
}
__device__ __forceinline__ void touch_sc1(unsigned& sink, rsrc_t r, unsigned voff, unsigned soff) {
    asm volatile("buffer_load_dword %0, %1, %2, %3 offen sc1" : "+v"(sink) : "v"(voff), "s"(r), "s"(soff) : "memory");
}
__device__ __forceinline__ bf16x8 lds_b128(unsigned p) { return *(LAS bf16x8*)(size_t)p; }
__device__ __forceinline__ s16x4 lds_tr(unsigned p) { return __builtin_amdgcn_ds_read_tr16_b64_v4i16((LAS s16x4*)(size_t)p); }
__device__ __forceinline__ bf16x8 cat8(s16x4 a, s16x4 b) { return __builtin_bit_cast(bf16x8, __builtin_shufflevector(a, b, 0, 1, 2, 3, 4, 5, 6, 7)); }
__device__ __forceinline__ bf16x8 lds_tr2(unsigned p0, unsigned p1) { return cat8(lds_tr(p0), lds_tr(p1)); }
__device__ __forceinline__ unsigned pack_bf16(float a, float b) {
    bf16x2 v = {(__bf16)a, (__bf16)b};
    return __builtin_bit_cast(unsigned, v);
}
__device__ __forceinline__ u32x4 swiglu8(f32x4 g0, f32x4 g1, f32x4 u0, f32x4 u1) {
    const f32x2 g[4] = {{g0[0], g0[1]}, {g0[2], g0[3]}, {g1[0], g1[1]}, {g1[2], g1[3]}};
    const f32x2 u[4] = {{u0[0], u0[1]}, {u0[2], u0[3]}, {u1[0], u1[1]}, {u1[2], u1[3]}};
    f32x2 t[4];
#pragma unroll
    for (int k = 0; k < 4; ++k) t[k] = g[k] * -1.44269504088896341f;
#pragma unroll
    for (int k = 0; k < 4; ++k) { t[k][0] = __builtin_amdgcn_exp2f(t[k][0]); t[k][1] = __builtin_amdgcn_exp2f(t[k][1]); }
#pragma unroll
    for (int k = 0; k < 4; ++k) t[k] += 1.0f;
#pragma unroll
    for (int k = 0; k < 4; ++k) { t[k][0] = __builtin_amdgcn_rcpf(t[k][0]); t[k][1] = __builtin_amdgcn_rcpf(t[k][1]); }
#pragma unroll
    for (int k = 0; k < 4; ++k) t[k] *= g[k];
#pragma unroll
    for (int k = 0; k < 4; ++k) t[k] *= u[k];
    return (u32x4){pack_bf16(t[0][0], t[0][1]), pack_bf16(t[1][0], t[1][1]), pack_bf16(t[2][0], t[2][1]), pack_bf16(t[3][0], t[3][1])};
}
__device__ __forceinline__ u64 mix64(u64 z) {
    z += 0x9e3779b97f4a7c15ull;
    z = (z ^ (z >> 30)) * 0xbf58476d1ce4e5b9ull;
    z = (z ^ (z >> 27)) * 0x94d049bb133111ebull;
    return z ^ (z >> 31);
}
__device__ __forceinline__ u64 next_tag(u64 e) { return (mix64(e) | 0x01010101ull) ^ (e & 0xffffffffull); }
__device__ __forceinline__ unsigned key_a(u64 tag) { return (unsigned)(tag >> 32); }
__device__ __forceinline__ unsigned key_b(u64 tag) { return (unsigned)(tag >> 32) * 0x9e3779b1u + 1u; }
__device__ __forceinline__ unsigned key_c(u64 tag) { return (unsigned)tag; }
__device__ __forceinline__ u64 sload64(const char* p) {
    u64 v;
    asm volatile("s_load_dwordx2 %0, %1, 0x0\n\ts_waitcnt lgkmcnt(0)" : "=&s"(v) : "s"(p) : "memory");
    return v;
}
__device__ __forceinline__ void flag_store(rsrc_t r, unsigned soff, u64 tag, int lane) {
    const u32x2 v = {(unsigned)tag, (unsigned)(tag >> 32)};
    __builtin_amdgcn_raw_buffer_store_b64(v, r, (lane & 7) * INBOX, soff, 16);
}
__device__ __forceinline__ bool flag_ok(u32x2 v, u64 tag) { return v[0] == (unsigned)tag && v[1] == (unsigned)(tag >> 32); }
__device__ __forceinline__ void flag_wait(rsrc_t r, unsigned soff, unsigned vo, u64 tag) {
#pragma nounroll
    for (unsigned spins = 0; spins < SPIN_CAP; ++spins) {
        if (__all(flag_ok(__builtin_amdgcn_raw_buffer_load_b64(r, vo, soff, 16), tag))) break;
        __builtin_amdgcn_s_sleep(1);
        asm volatile("" ::: "memory");
    }
    asm volatile("" ::: "memory");
}
#define SGB(mask, n) __builtin_amdgcn_sched_group_barrier(mask, n, 0)
#define SB() __builtin_amdgcn_sched_barrier(0)
__device__ __forceinline__ void mfma_acc(f32x4& c, const bf16x8& a, const bf16x8& b) {
    asm volatile("v_mfma_f32_16x16x32_bf16 %0, %1, %2, %0" : "+v"(c) : "v"(a), "v"(b));
}
__device__ __forceinline__ void mfma_new(f32x4& c, const bf16x8& a, const bf16x8& b) {
    asm volatile("v_mfma_f32_16x16x32_bf16 %0, %1, %2, 0" : "=&v"(c) : "v"(a), "v"(b));
}
template <int V> struct IC { static constexpr int value = V; };
template <int I, int N, class F> __device__ __forceinline__ void static_for(F&& f) {
    if constexpr (I < N) { f(IC<I>{}); static_for<I + 1, N>(f); }
}

constexpr int G1_STAGE = 48 * 1024, G1_NK = 16;
constexpr int RING = 14336;
constexpr int L_WST = 144 * 1024;
__host__ __device__ constexpr int ring_off(int slot) { return (slot / 3) * 2 * G1_STAGE + (slot % 3) * RING; }
__host__ __device__ constexpr bool isL(int i) { return i % 4 == 2; }
__host__ __device__ constexpr int Lidx(int i) { return i / 4; }
__host__ __device__ constexpr int Ridx(int i) { return i - i / 4 - (i % 4 == 3 ? 1 : 0); }
__host__ __device__ constexpr int Rstep(int n) { return 4 * (n / 3) + (n % 3 == 2 ? 3 : n % 3); }
__host__ __device__ constexpr int Lstep(int l) { return 4 * l + 2; }
constexpr int BS = 4;
constexpr int F0 = 9, F1 = 13;
__host__ __device__ constexpr int fyoung(int t) { return (t > F0 + 3 && t <= F0 + 6 ? 3 : 0) + (t > F1 + 3 && t <= F1 + 6 ? 3 : 0); }
constexpr int WS0 = 3;
constexpr int NRIDE = 16 - WS0, NTA = 6, NTB = 21 - NRIDE - NTA;
static_assert(WS0 >= 2 && NTB >= 1 && NTB <= 5, "Wd schedule");

typedef u64 u64x4 __attribute__((ext_vector_type(4)));
typedef u64 u64x2 __attribute__((ext_vector_type(2)));
extern "C" __global__ __launch_bounds__(512, 2) void ffn_fused(const char* x_, const char* wg_, const char* wu_, const char* wd_,
                                                               char* out_, char* ws_) {
    u64x4 ka; u64x2 kb;
    asm volatile("s_load_dwordx8 %0, %2, 0x0\n\ts_load_dwordx4 %1, %2, 0x20" : "=&s"(ka), "=&s"(kb) : "s"(__builtin_amdgcn_kernarg_segment_ptr()));
    extern __shared__ __attribute__((aligned(16))) char smem_[];
    const unsigned lbase = (unsigned)(size_t)(LAS char*)smem_;
    const int tid = threadIdx.x, lane = tid & 63;
    const int wid = __builtin_amdgcn_readfirstlane(tid >> 6);
    unsigned vx[4], vw[4];
#pragma unroll
    for (int i = 0; i < 4; ++i) {
        const int r = 8 * i + (lane >> 3);
        vx[i] = r * 2048 + (((lane & 7) ^ ((r >> 1) & 7)) << 4) - i * 1024;
        const int f = (lane >> 4) | ((i >> 1) << 2);
        vw[i] = (4 * i + (lane >> 4)) * (FF * 2) + (((lane & 15) ^ (f << 1)) << 4) - i * 1024;
        asm volatile("" : "+v"(vx[i]), "+v"(vw[i]));
    }
    const int b = blockIdx.x;
    const int xcd = b & 7, idx = b >> 3;
    const int mb = idx & 7, j = idx >> 3;
    const int kq = xcd >> 1, hi = xcd & 1, nb = hi * 4 + j;
    const bool helper = (j == 3) && ((mb >> 2) != hi);
    const int fb = 7 * kq + (j == 3 ? 3 : nb);
    const int tile = mb * 8 + nb;
    auto quarter = [&](int pos) { return (kq + 1 + pos) & 3; };
    unsigned xbase = (mb * 128 + quarter((wid & 1) * 2 + ((wid >> 1) & 1)) * 32) * 2048, wbase = ((wid & 3) * 16) * (FF * 2) + fb * 256;
    unsigned ldst = lbase + (wid & 3) * 4096;
    const bool loader = wid >= 4 && !helper;
    asm volatile("" : "+s"(xbase), "+s"(wbase), "+s"(ldst));
    asm volatile("s_waitcnt lgkmcnt(0)" : "+s"(ka), "+s"(kb));
    const char *x = (const char*)ka[0], *wg = (const char*)ka[1], *wu = (const char*)ka[2], *wd = (const char*)ka[3];
    char *out = (char*)kb[0], *ws = (char*)kb[1];
    const rsrc_t rx = __builtin_amdgcn_make_buffer_rsrc((void*)x, 0, MTOK * DM * 2, 0x00020000);
    const rsrc_t rg = __builtin_amdgcn_make_buffer_rsrc((void*)wg, 0, DM * FF * 2, 0x00020000);
    const rsrc_t ru = __builtin_amdgcn_make_buffer_rsrc((void*)wu, 0, DM * FF * 2, 0x00020000);
    auto kord = [&](int s) { return (s & ~(BS - 1)) | ((s + mb) & (BS - 1)); };
    auto stage_g = [&](int s) { dma4(rg, ldst + (s % 3) * G1_STAGE + 16384, vw, wbase + kord(s) * 64 * (FF * 2)); };
    auto stage_u = [&](int s) { dma4(ru, ldst + (s % 3) * G1_STAGE + 32768, vw, wbase + kord(s) * 64 * (FF * 2)); };
    auto stage_x = [&](int s) { dma4(rx, ldst + (s % 3) * G1_STAGE, vx, xbase + kord(s) * 128); };
    if (__builtin_expect(loader, 1)) {
        stage_x(0); stage_g(0); stage_u(0);
        stage_x(1); stage_g(1); stage_u(1);
    }
    SB();
    const rsrc_t rws = __builtin_amdgcn_make_buffer_rsrc((void*)ws, 0, WS_BYTES, 0x00020000);
    const rsrc_t rd_ = __builtin_amdgcn_make_buffer_rsrc((void*)wd, 0, DM * FF * 2, 0x00020000);
    const int g = lane >> 4, q = (lane & 15) >> 2, pp = lane & 3;

    if (__builtin_expect(wid >= 4, 1)) {
        const int p = wid - 4;
        const unsigned vwd0 = (lane >> 2) * (DM * 2) + (((lane & 3) ^ (((lane >> 4) & 1) << 1)) << 4);
        const unsigned vwd1 = vwd0 + 16 * (DM * 2) - 1024;
        const unsigned wdbase = kq * 896 * (DM * 2) + nb * 256 + p * 64;
        auto wd_step = [&](int i, unsigned dst) { dma2(rd_, dst, vwd0, vwd1, wdbase + i * 32 * (DM * 2)); };
        const unsigned wst = lbase + L_WST + p * 4096, wres = lbase + G1_STAGE + p * 12288;
        unsigned at[2];
#pragma unroll
        for (int nt = 0; nt < 2; ++nt) at[nt] = (4 * g + q) * 64 + ((nt ^ (g & 1)) << 5) + pp * 8;
        bf16x8 wr[21][2];
        auto wd_read = [&](int n, int cnt, unsigned src) {
#pragma unroll
            for (int e = 0; e < cnt; ++e)
#pragma unroll
                for (int nt = 0; nt < 2; ++nt) wr[n + e][nt] = lds_tr2(src + at[nt] + e * 2048, src + at[nt] + e * 2048 + 1024);
            SB();
        };
        auto wd_done = [&](int n, int cnt) {
            asm volatile("s_waitcnt lgkmcnt(0)" : "+v"(wr[n][0]), "+v"(wr[n][1])::"memory");
#pragma unroll
            for (int e = 1; e < cnt; ++e) asm volatile("" : "+v"(wr[n + e][0]), "+v"(wr[n + e][1])::"memory");
        };
        auto wd_issue = [&](int n, int cnt, unsigned dst) {
#pragma unroll
            for (int e = 0; e < cnt; ++e) wd_step(Rstep(n + e), dst + e * 2048);
        };
        const unsigned wta = lbase + 2 * G1_STAGE + p * 12288;
        u64 tag;
        if (__builtin_expect(!helper, 1)) {
            tag = next_tag(sload64(ws + WS_EPOCH + mb * 256));
            asm volatile("" : "+s"(tag));
            wait_vm<12>();
            barrier();
#pragma unroll
            for (int s = 2; s < G1_NK; ++s) {
                stage_x(s); stage_g(s); stage_u(s);
                if (s > WS0 + 1) wd_done(s - 2 - WS0, 1);
                if (s >= WS0) { wd_issue(s - WS0, 1, wst + (s & 1) * 2048); wait_vm<14>(); } else wait_vm<12>();
                if (s > WS0) wd_read(s - 1 - WS0, 1, wst + ((s - 1) & 1) * 2048);
                barrier();
            }
#pragma unroll
            for (int l = 0; l < 6; ++l) wd_step(Lstep(l), wres + l * 2048);
            wait_vm<12>();
            barrier();
            wd_read(NRIDE - 1, 1, wst + 2048);
            wd_issue(NRIDE, NTA, wta);
            wd_done(NRIDE - 2, 2);
            wd_issue(NRIDE + NTA, 1, wst + 2048);
            wd_step(Lstep(6), wst);
            wait_vm<2>();
            wd_read(NRIDE, NTA, wta);
            wd_read(NRIDE + NTA, 1, wst + 2048);
            wd_done(NRIDE, NTA + 1);
            if (NTB > 1) {
                wd_issue(NRIDE + NTA + 1, NTB - 1, wta);
                wait_vm<0>();
                wd_read(NRIDE + NTA + 1, NTB - 1, wta);
                wd_done(NRIDE + NTA + 1, NTB - 1);
            }
        } else {
            tag = next_tag(sload64(ws + WS_EPOCH + mb * 256));
            asm volatile("" : "+s"(tag));
            barrier();
            const unsigned hst = lbase + p * 12288;
            wd_issue(0, 6, hst); wd_issue(6, 6, wta);
            wait_vm<12>();
            wd_read(0, 6, hst); wd_done(0, 6);
            wd_issue(12, 6, hst);
            wait_vm<12>();
            wd_read(6, 6, wta); wd_done(6, 6);
            wd_issue(18, 3, wta);
#pragma unroll
            for (int l = 0; l < 6; ++l) wd_step(Lstep(l), wres + l * 2048);
            wd_step(Lstep(6), wst);
            wait_vm<14>();
            wd_read(12, 6, hst); wd_read(18, 3, wta); wd_done(12, 9);
        }
        barrier();
        const unsigned hfa = WS_HFLAG + (mb * 4 + kq) * 8 * INBOX;
        if (p == 2) flag_store(rws, hfa + nb * 8, tag, lane);
        if (p < 2) {
            flag_wait(rws, hfa + nb * INBOX, (lane & 7) * 8, tag);
            const unsigned rowb = WS_H + ((mb * 8 + 2 * quarter(0) + p) * 112 + kq * 28) * 1024;
#pragma unroll
            for (int t = 0; t < 3; ++t) dma7_sc1(rws, lbase + t * RING + p * 7168, lane * 16, rowb + t * 7168);
        } else {
            wait_vm<0>();
        }
        const unsigned awl[2] = {wres + at[0], wres + at[1]}, awl6[2] = {wst + at[0], wst + at[1]};
        unsigned hl[2] = {lbase + lane * 16, lbase + lane * 16 + ring_off(3)};
        asm volatile("" : "+v"(hl[1]));
        auto hread = [&](int gs, int rt) {
            const int slot = (gs / 7) % 6;
            return lds_b128(hl[slot / 3] + (slot % 3) * RING + rt * 7168 + (gs % 7) * 1024);
        };
        bf16x8 hf[4][2];
        s16x4 wt[2][2];
        f32x4 acc[2][4];
        unsigned pso[3];
#pragma unroll
        for (int qi = 0; qi < 3; ++qi) {
            pso[qi] = WS_PART + (((tile * 4 + quarter(qi)) * 3 + qi) * 4 + p) * PBLK;
            asm volatile("" : "+s"(pso[qi]));
        }
        const unsigned ka_ = __builtin_amdgcn_readfirstlane(key_a(tag)), kb_ = __builtin_amdgcn_readfirstlane(key_b(tag)), kc_ = __builtin_amdgcn_readfirstlane(key_c(tag));
        u32x4 pk[2];
        unsigned chk;
        wt[0][0] = lds_tr(awl[0]); wt[0][1] = lds_tr(awl[0] + 1024);
        wait_vm<14>();
        barrier();
#pragma unroll
        for (int i = 0; i < 3; ++i) { hf[i][0] = hread(i, 0); hf[i][1] = hread(i, 1); }
        static_for<0, 112>([&](auto GS) __attribute__((always_inline)) {
            constexpr int gs = decltype(GS)::value;
            constexpr int qi = gs / 28, i = gs % 28, nx = gs + 3;
            constexpr int l3 = (i + 3) % 28, l2 = (i + 2) % 28;
            constexpr int lf = isL(l3) ? l3 : isL(l2) ? l2 : -1, lnt = isL(l3) ? 0 : 1;
            constexpr bool more = nx < 112;
            const bf16x8 w0 = isL(i) ? cat8(wt[0][0], wt[0][1]) : wr[isL(i) ? 0 : Ridx(i)][0];
            const bf16x8 w1 = isL(i) ? cat8(wt[1][0], wt[1][1]) : wr[isL(i) ? 0 : Ridx(i)][1];
            f32x4(&c)[4] = acc[qi & 1];
            bf16x8 hn0, hn1;
            f32x4(&d)[4] = acc[(qi + 1) & 1];
            auto mark = [&](int k) {
                if constexpr (qi > 0 && i < 2) {
                    constexpr int h = 1 - i;
                    unsigned w = pack_bf16(d[2 * h + (k >> 1)][2 * (k & 1)], d[2 * h + (k >> 1)][2 * (k & 1) + 1]);
                    if (k == 0) w ^= h ? kb_ : ka_;
                    asm volatile("" : "+v"(w));
                    pk[h][k] = w;
                }
                if constexpr (qi > 0 && i == 2) {
                    chk = __builtin_amdgcn_bitop3_b32(k == 0 ? kc_ : chk, pk[0][k], pk[1][k], 0x96);
                    asm volatile("" : "+v"(chk));
                }
            };
            SB();
            if (i == 0) mfma_new(c[2], w0, hf[gs % 4][1]); else mfma_acc(c[2], w0, hf[gs % 4][1]);
            SB();
            if constexpr (nx == 7 || nx == 14) wait_vm<14 - nx>();
            if (more && nx % 7 == 0) barrier();
            if (more) hn0 = hread(nx, 0);
            if (i > 0) mark(0);
            SB();
            if (i == 0) mfma_new(c[0], w0, hf[gs % 4][0]); else mfma_acc(c[0], w0, hf[gs % 4][0]);
            SB();
            if (more) hn1 = hread(nx, 1);
            if (i == 0) mark(0);
            mark(1);
            SB();
            if (i == 0) mfma_new(c[3], w1, hf[gs % 4][1]); else mfma_acc(c[3], w1, hf[gs % 4][1]);
            SB();
            if (lf >= 0 && gs < 110) wt[lnt][0] = lds_tr(Lidx(lf) < 6 ? awl[lnt] + Lidx(lf) * 2048 : awl6[lnt]);
            mark(2); mark(3);
            SB();
            if (i == 0) mfma_new(c[1], w1, hf[gs % 4][0]); else mfma_acc(c[1], w1, hf[gs % 4][0]);
            SB();
            if constexpr (qi > 0 && i < 2) store16_sc1<(1 - i) * 1024>(pk[1 - i], rws, lane * 16, pso[qi - 1]);
            if constexpr (qi > 0 && i == 2) __builtin_amdgcn_raw_buffer_store_b32(chk, rws, lane * 4 + 2048, pso[qi - 1], 16);
            if (lf >= 0 && gs < 110) wt[lnt][1] = lds_tr((Lidx(lf) < 6 ? awl[lnt] + Lidx(lf) * 2048 : awl6[lnt]) + 1024);
            if (more) { hf[nx % 4][0] = hn0; hf[nx % 4][1] = hn1; }
            SB();
        });
        asm volatile("s_nop 7\n\ts_nop 1" : "+v"(acc[1][0]), "+v"(acc[1][1]), "+v"(acc[1][2]), "+v"(acc[1][3]));
#pragma unroll
        for (int e = 0; e < 4; ++e) *(LAS f32x4*)(size_t)(lbase + p * 4096 + e * 1024 + lane * 16) = acc[1][e];
        wait_lgkm0();
        barrier();
        asm volatile(".Lffn_end0:");
    } else {
        u64 tag;
        const unsigned hfl = WS_HFLAG + (mb * 4 + kq) * 8 * INBOX, myflag = hfl + INB_B + (nb * 4 + wid) * 8;
        if (__builtin_expect(!helper, 1)) {
            tag = next_tag(sload64(ws + WS_EPOCH + mb * 256));
            asm volatile("" : "+s"(tag));
            const int wm = wid >> 1, wn = wid & 1;
            const int fr = lane & 15;
            unsigned ax[2], aw[4];
#pragma unroll
            for (int ks = 0; ks < 2; ++ks) ax[ks] = lbase + (wm * 64 + fr) * 128 + ((((ks * 4 + g) ^ (fr >> 1)) & 7) << 4);
            const int fsw = q | ((g & 1) << 2);
#pragma unroll
            for (int nt = 0; nt < 4; ++nt) aw[nt] = lbase + 16384 + (8 * g + q) * 256 + (((wn * 4 + nt) ^ fsw) << 5) + pp * 8;
            f32x4 acc[4][8];
#pragma unroll
            for (int i = 0; i < 4; ++i)
#pragma unroll
                for (int jj = 0; jj < 8; ++jj) { acc[i][jj] = (f32x4){0.f, 0.f, 0.f, 0.f}; asm volatile("" : "+v"(acc[i][jj])); }
            bf16x8 xa[4], wa[8], xb[4], wb[8];
            auto rd = [&](bf16x8(&xf)[4], bf16x8(&wf)[8], int ks) {
#pragma unroll
                for (int nt = 0; nt < 8; ++nt) {
                    unsigned pw = aw[nt & 3] + (nt >> 2) * 16384 + ks * 8192;
                    wf[nt] = lds_tr2(pw, pw + 1024);
                    if (nt < 4) xf[nt] = lds_b128(ax[ks] + nt * 2048);
                }
            };
            auto mm = [&](bf16x8(&xf)[4], bf16x8(&wf)[8]) {
#pragma unroll
                for (int nt = 0; nt < 8; ++nt)
#pragma unroll
                    for (int m = 0; m < 4; ++m) {
                        const int mt = (nt & 1) ? 3 - m : m;
                        acc[mt][nt] = __builtin_amdgcn_mfma_f32_16x16x32_bf16(wf[nt], xf[mt], acc[mt][nt], 0, 0, 0);
                    }
            };
            SB();
            barrier();
            rd(xa, wa, 0);
            wait_lgkm0();
            int slot = 0;
            for (int s = 0; s < G1_NK - 1; ++s) {
                rd(xb, wb, 1);
                mm(xa, wa);
                const int d = slot == 2 ? -2 * G1_STAGE : G1_STAGE;
                slot = slot == 2 ? 0 : slot + 1;
#pragma unroll
                for (int i = 0; i < 2; ++i) ax[i] += d;
#pragma unroll
                for (int i = 0; i < 4; ++i) aw[i] += d;
#pragma unroll
                for (int i = 0; i < 20; ++i) { SGB(0x8, 1); SGB(0x100, 1); }
#pragma unroll
                for (int i = 0; i < 6; ++i) { SGB(0x8, 1); SGB(0x2, 1); }
                SGB(0x8, 6);
                SB();
                wait_lgkm0();
                barrier();
                SB();
                rd(xa, wa, 0);
                mm(xb, wb);
#pragma unroll
                for (int i = 0; i < 20; ++i) { SGB(0x8, 1); SGB(0x100, 1); }
                SGB(0x8, 12);
                SB();
                wait_lgkm0();
                SB();
            }
            rd(xb, wb, 1);
            mm(xa, wa);
#pragma unroll
            for (int i = 0; i < 20; ++i) { SGB(0x8, 1); SGB(0x100, 1); }
            SGB(0x8, 12);
            SB();
            const unsigned hso[2] = {WS_H + ((mb * 8 + 2 * quarter(wm)) * 112 + fb * 4 + wn * 2) * 1024, WS_H + ((mb * 8 + 2 * quarter(2 + wm)) * 112 + fb * 4 + wn * 2) * 1024};
            auto mrow = [&](int mt) {
#pragma unroll
                for (int nt = 0; nt < 8; ++nt) acc[mt][nt] = __builtin_amdgcn_mfma_f32_16x16x32_bf16(wb[nt], xb[mt], acc[mt][nt], 0, 0, 0);
            };
            mrow(0);
            SB();
#pragma unroll
            for (int mt = 0; mt < 4; ++mt) {
                if (mt < 3) mrow(mt + 1);
#pragma unroll
                for (int s2 = 0; s2 < 2; ++s2) {
                    const u32x4 o = swiglu8(acc[mt][2 * s2], acc[mt][2 * s2 + 1], acc[mt][4 + 2 * s2], acc[mt][5 + 2 * s2]);
                    const unsigned so = hso[mt >> 1] + (mt & 1) * 112 * 1024;
                    if (s2 == 0) store16_sc1<0>(o, rws, lane * 16, so); else store16_sc1<1024>(o, rws, lane * 16, so);
                }
                if (mt < 3) {
#pragma unroll
                    for (int i = 0; i < 8; ++i) { SGB(0x8, 1); SGB(0x402, 4); }
                }
                SB();
            }
            wait_vm<4>();
            barrier();
            wait_vm<0>();
            flag_store(rws, myflag, tag, lane);
        } else {
            unsigned sink = 0;
            const int hw = (mb & 3) * 4 + wid;
            const unsigned vt = (lane >> 3) * 2048 + (lane & 7) * 128, vf = (lane >> 3) * (FF * 2) + (lane & 7) * 128;
            const unsigned tx = (xcd * 16 + hw) * 8 * 2048, td = (kq * 896 + hw * 8) * (DM * 2) + hi * 1024;
            const unsigned tw = hw * 8 * (FF * 2) + (7 * kq + 3 * hi) * 256;
            auto tD = [&](int e) { touch(sink, rd_, vt, td + e * 128 * (DM * 2)); };
            auto tW = [&](int blk) {
#pragma unroll
                for (int e = 0; e < 2; ++e) { touch(sink, rg, vf, tw + (blk * 256 + e * 128) * (FF * 2)); touch(sink, ru, vf, tw + (blk * 256 + e * 128) * (FF * 2)); }
            };
            u64 pc; unsigned back, e0, e1;
            asm volatile("s_getpc_b64 %0\n.Lffn_pc:\n\ts_mov_b32 %1, .Lffn_pc-ffn_fused\n\ts_mov_b32 %2, .Lffn_end0-ffn_fused\n\ts_mov_b32 %3, .Lffn_end1-ffn_fused"
                         : "=s"(pc), "=s"(back), "=s"(e0), "=s"(e1));
            const rsrc_t rc = __builtin_amdgcn_make_buffer_rsrc((void*)(pc - back), 0, e0 > e1 ? e0 : e1, 0x00020000);
            if ((mb & 3) == 1) touch(sink, rc, tid * 128, 0);
            touch(sink, rx, vt, tx); tD(0); touch(sink, rx, vt, tx + 1024); tD(1);
            tW(2);
#pragma unroll
            for (int e = 0; e < 2; ++e) { const int c = hw * 2 + e; touch_sc1(sink, rws, (lane & 3) * 128, WS_HFLAG + (((c & 7) * 4 + kq) * 8 + hi * 4 + (c >> 3)) * INBOX); }
            tD(2); tD(3);
            tW(3);
            tD(4); tD(5); tD(6);
            if ((mb & 3) == 0) touch(sink, rc, tid * 128, 0);
            tag = next_tag(sload64(ws + WS_EPOCH + mb * 256));
            asm volatile("" : "+s"(tag));
            asm volatile("s_waitcnt vmcnt(0)" : "+v"(sink)::"memory");
            barrier();
            flag_store(rws, myflag, tag, lane);
            if (wid == 0) flag_store(rws, hfl + nb * 8, tag, lane);
            barrier();
        }
        const unsigned pd = WS_PART + ((tile * 4 + kq) * 3 * 4 + wid) * PBLK;
        u32x4 v[3][2];
        unsigned vc[3];
        auto fetch = [&](int k) {
            v[k][1] = __builtin_amdgcn_raw_buffer_load_b128(rws, lane * 16 + 1024, pd + k * 4 * PBLK, 16);
            v[k][0] = __builtin_amdgcn_raw_buffer_load_b128(rws, lane * 16, pd + k * 4 * PBLK, 16);
            vc[k] = __builtin_amdgcn_raw_buffer_load_b32(rws, lane * 4 + 2048, pd + k * 4 * PBLK, 16);
        };
        auto early = [&](int gi) { if (gi == F0) fetch(0); if (gi == F1) fetch(1); };
        auto valid = [&](int k) {
            const u32x4 t = v[k][0] ^ v[k][1];
            return __all((t[0] ^ t[1] ^ t[2] ^ t[3] ^ vc[k]) == key_c(tag)) != 0;
        };
        if (wid < 2) {
            const unsigned vh = lane * 16;
            auto hstage = [&](int t, int slot) {
                const int qq = quarter(t >> 2);
                const unsigned rowb = WS_H + ((mb * 8 + 2 * qq + wid) * 112 + kq * 28) * 1024;
                dma7_sc1(rws, lbase + ring_off(slot) + wid * 7168, vh, rowb + (t & 3) * 7168);
            };
            barrier();
            hstage(3, 3);
            static_for<4, 19>([&](auto T) __attribute__((always_inline)) {
                constexpr int t = decltype(T)::value;
                if constexpr (t < 16) hstage(t, t % 6);
                constexpr int nst = (t < 16 ? t : 15) - (t - 3);
                wait_vm<7 * nst + fyoung(t)>();
                barrier();
                early(t - 3);
            });
        } else {
            barrier(); barrier();
            const u32x2 fb0 = __builtin_amdgcn_raw_buffer_load_b64(rws, (lane & 31) * 8, hfl + nb * INBOX + INB_B, 16);
            barrier(); barrier();
            if (!__all(flag_ok(fb0, tag))) flag_wait(rws, hfl + nb * INBOX + INB_B, (lane & 31) * 8, tag);
#pragma unroll
            for (int t = 4; t < 16; ++t) { barrier(); early(t); }
        }
        fetch(2);
        bool h0 = valid(0), h1 = valid(1), h2 = valid(2);
#pragma nounroll
        for (unsigned spins = 0; spins < SPIN_CAP && !(h0 && h1 && h2); ++spins) {
            asm volatile("" ::: "memory");
            if (!h0) fetch(0);
            if (!h1) fetch(1);
            if (!h2) fetch(2);
            h0 = valid(0); h1 = valid(1); h2 = valid(2);
        }
        f32x4 r[4];
#pragma unroll
        for (int e = 0; e < 4; ++e) {
            r[e] = (f32x4){0.f, 0.f, 0.f, 0.f};
#pragma unroll
            for (int k = 0; k < 3; ++k) {
                const unsigned w0 = v[k][e >> 1][2 * (e & 1)] ^ ((e & 1) ? 0u : (e >> 1) ? key_b(tag) : key_a(tag)), w1 = v[k][e >> 1][2 * (e & 1) + 1];
                r[e] += __builtin_bit_cast(f32x4, (u32x4){w0 << 16, w0 & 0xffff0000u, w1 << 16, w1 & 0xffff0000u});
            }
        }
        const rsrc_t ro = __builtin_amdgcn_make_buffer_rsrc((void*)out, 0, MTOK * DM * 2, 0x00020000);
        unsigned op = (mb * 128 + kq * 32 + (lane & 15)) * (DM * 2) + (nb * 128 + wid * 32 + (g & 1) * 16 + (g >> 1) * 8) * 2;
        asm volatile("" : "+v"(r[0]), "+v"(r[1]), "+v"(r[2]), "+v"(r[3]), "+v"(op));
        barrier();
#pragma unroll
        for (int e = 0; e < 4; ++e) r[e] += *(LAS f32x4*)(size_t)(lbase + wid * 4096 + e * 1024 + lane * 16);
#pragma unroll
        for (int mt = 0; mt < 2; ++mt) {
            const auto s0 = __builtin_amdgcn_permlane16_swap(pack_bf16(r[2 * mt][0], r[2 * mt][1]), pack_bf16(r[2 * mt + 1][0], r[2 * mt + 1][1]), false, false);
            const auto s1 = __builtin_amdgcn_permlane16_swap(pack_bf16(r[2 * mt][2], r[2 * mt][3]), pack_bf16(r[2 * mt + 1][2], r[2 * mt + 1][3]), false, false);
            store16((u32x4){s0[0], s1[0], s0[1], s1[1]}, ro, op, mt * 16 * (DM * 2));
        }
        if (wid == 2 && nb == 0 && kq == 0) __builtin_amdgcn_raw_buffer_store_b64((u32x2){(unsigned)tag, (unsigned)(tag >> 32)}, rws, 0, WS_EPOCH + mb * 256, 16);
        asm volatile(".Lffn_end1:");
    }
}

extern "C" void kernel_launch(void* const* d_in, const int* in_sizes, int n_in, void* d_out, int out_size, void* d_ws,
                              size_t ws_size, hipStream_t stream) {
    (void)hipFuncSetAttribute((const void*)ffn_fused, hipFuncAttributeMaxDynamicSharedMemorySize, LDS_BYTES);
    ffn_fused<<<256, 512, LDS_BYTES, stream>>>((const char*)d_in[0], (const char*)d_in[1], (const char*)d_in[2],
                                               (const char*)d_in[3], (char*)d_out, (char*)d_ws);
}
```
